```python
import torch

def _softmax(x, axis=-1):
    return torch.softmax(x, dim=axis)
def _gelu(x):
    return torch.nn.functional.gelu(x, approximate="tanh")
def _silu(x):
    return torch.nn.functional.silu(x)
def _sigmoid(x):
    return torch.sigmoid(x)
def _layernorm(x, axis=-1, eps=1e-5):
    m = x.mean(dim=axis, keepdim=True)
    v = x.var(dim=axis, keepdim=True, unbiased=False)
    return (x - m) / torch.sqrt(v + eps)
def _scatter_add(tgt, idx, src, axis=0):
    return tgt.clone().index_add_(axis, idx, src)
def _segment_sum(src, seg, n):
    out = torch.zeros((n,) + src.shape[1:], dtype=src.dtype)
    return out.index_add_(0, seg, src)
def _pad(x, pw):
    flat = tuple(v for pair in reversed(pw) for v in pair)
    return torch.nn.functional.pad(x, flat)

kernel_name = 'gated_mix__sliding_local_dilated__softmax_full_mha__prompt'

def setup_inputs():

    _w = (lambda a: (lambda t: t.to(torch.bfloat16) if t.is_floating_point() else t)(torch.as_tensor(a)))
    def _A_setup():
        B, S, D = 1, 2048, 512
        return {
            "x": _w((lambda *s: torch.randn(*s, dtype=torch.bfloat16) * 0.05)(B, S, D)),
            "wqkv": _w((lambda *s: torch.randn(*s, dtype=torch.bfloat16) * 1.6)(D, 3 * D)),
            "wo": _w((lambda *s: torch.randn(*s, dtype=torch.bfloat16) * 0.05)(D, D)),
        }
    _A_in = _A_setup()

    def _B_setup():
        B, S, D = 1, 2048, 512
        return {
            "x": _w((lambda *s: torch.randn(*s, dtype=torch.bfloat16) * 0.05)(B, S, D)),
            "wqkv": _w((lambda *s: torch.randn(*s, dtype=torch.bfloat16) * 1.6)(D, 3 * D)),
            "wo": _w((lambda *s: torch.randn(*s, dtype=torch.bfloat16) * 0.1)(D, D)),
        }
    _B_in = _B_setup()

    return {
        "A_x": _A_in["x"],
        "A_wqkv": _A_in["wqkv"],
        "A_wo": _A_in["wo"],
        "B_x": _B_in["x"],
        "B_wqkv": _B_in["wqkv"],
        "B_wo": _B_in["wo"],
        "gate_w": _w((lambda *s: torch.randn(*s, dtype=torch.bfloat16) * 0.05)(512) * 16.0),
    }

def reference(A_x, A_wqkv, A_wo, B_x, B_wqkv, B_wo, gate_w):
    def _A_ref(x, wqkv, wo):
        B, S, D = x.shape
        H = 32; Dh = D // H
        W, dil = 8, 2
        qkv = (lambda x, *d: x.permute(*d))((x @ wqkv).reshape(B, S, 3, H, Dh), 2, 0, 3, 1, 4)
        q, k, v = qkv[0], qkv[1], qkv[2]
        scale = Dh ** -0.5
        pos = torch.arange(S)
        off = torch.arange(W) * dil
        idx = pos[:, None] - off[None, :]
        mask = idx >= 0
        idx_c = torch.where(mask, idx, 0).reshape(-1)
        kg = k[:, :, idx_c, :].reshape(B, H, S, W, Dh)
        vg = v[:, :, idx_c, :].reshape(B, H, S, W, Dh)
        logits = torch.einsum("bhsd,bhswd->bhsw", q, kg) * scale
        logits = torch.where(mask[None, None, :, :], logits, -1e9)
        attn = _softmax(logits, axis=-1)
        o = torch.einsum("bhsw,bhswd->bhsd", attn, vg)
        return (lambda x, *d: x.permute(*d))(o, 0, 2, 1, 3).reshape(B, S, D) @ wo

    def _B_ref(x, wqkv, wo):
        B, S, D = x.shape
        H = 32; Dh = D // H
        qkv = (lambda x, *d: x.permute(*d))((x @ wqkv).reshape(B, S, 3, H, Dh), 2, 0, 3, 1, 4)
        q, k, v = qkv[0], qkv[1], qkv[2]
        scale = Dh ** -0.5
        scores = torch.einsum("bhqd,bhkd->bhqk", q, k) * scale
        p = _softmax(scores, axis=-1)
        o = torch.einsum("bhqk,bhkd->bhqd", p, v)
        return (lambda x, *d: x.permute(*d))(o, 0, 2, 1, 3).reshape(B, S, D) @ wo

    x = A_x
    g = _sigmoid((x * gate_w).sum(-1, keepdims=True))
    a = _A_ref(x, A_wqkv, A_wo)
    b = _B_ref(x, B_wqkv, B_wo)
    return g * a + (1 - g) * b
```

```cpp
#include <hip/hip_runtime.h>
#include <hip/hip_bf16.h>
#include <stdint.h>

typedef __attribute__((ext_vector_type(8))) __bf16 bf16x8;
typedef __attribute__((ext_vector_type(16))) __bf16 bf16x16;
typedef __attribute__((ext_vector_type(4))) short s16x4;
typedef __attribute__((ext_vector_type(4))) float f32x4;
typedef __attribute__((ext_vector_type(8))) float f32x8;
typedef __attribute__((ext_vector_type(16))) float f32x16;
typedef __attribute__((ext_vector_type(2))) unsigned u32x2;
typedef __attribute__((ext_vector_type(4))) unsigned u32x4;
typedef __attribute__((address_space(3))) const char* lds_cptr;
typedef __attribute__((address_space(3))) char* lds_ptr;

constexpr int SEQ = 2048, DM = 512, NH = 32;
constexpr float QSCALE = 0.25f * 1.4426950408889634f;

constexpr size_t WS_Q = 0;
constexpr size_t WS_K = WS_Q + (4u << 20);
constexpr size_t WS_V = WS_K + (4u << 20);
constexpr size_t WS_O = WS_V + (4u << 20);
constexpr size_t WS_W = WS_O + (4u << 20);
constexpr size_t WS_G = WS_W + (1u << 20);

#define PIN(x) asm volatile("" : "+v"(x))
#define SBAR() __builtin_amdgcn_sched_barrier(0)

__device__ __forceinline__ unsigned cvtpk(float lo, float hi) {
    typedef __attribute__((ext_vector_type(2))) float f32x2_; typedef __attribute__((ext_vector_type(2))) __bf16 bf16x2_;
    return __builtin_bit_cast(unsigned, __builtin_convertvector(f32x2_{lo, hi}, bf16x2_));
}
__device__ __forceinline__ float bf16r(float x) { return __uint_as_float(cvtpk(x, x) << 16); }
__device__ __forceinline__ float bflo(unsigned u) { return __uint_as_float(u << 16); }
__device__ __forceinline__ float bfhi(unsigned u) { return __uint_as_float(u & 0xffff0000u); }
__device__ __forceinline__ void glds16(const void* g, unsigned lds_base) {
    unsigned sv;
    asm volatile("s_mov_b32 %0, m0\n\ts_mov_b32 m0, %2\n\ts_nop 0\n\tglobal_load_lds_dwordx4 %1, off\n\ts_mov_b32 m0, %0"
                 : "=&s"(sv) : "v"(g), "s"(lds_base) : "memory");
}
__device__ __forceinline__ u32x4 gload16_asm(const void* g) {
    u32x4 r; asm volatile("global_load_dwordx4 %0, %1, off" : "=v"(r) : "v"(g) : "memory"); return r;
}
__device__ __forceinline__ bf16x8 lds_ld16(lds_cptr p) { return *(const __attribute__((address_space(3))) bf16x8*)p; }
__device__ __forceinline__ s16x4 lds_tr(lds_cptr p) {
    return __builtin_amdgcn_ds_read_tr16_b64_v4i16((__attribute__((address_space(3))) s16x4*)p);
}
__device__ __forceinline__ int crow(int r, int hi) { return (r & 3) + 8 * (r >> 2) + 4 * hi; }

constexpr int K1_STAGE = 40960, K1_XB = 16384, K1_NST = 4;
__global__ __launch_bounds__(512, 2) void k1_qkv(const __hip_bfloat16* __restrict__ x, const __hip_bfloat16* __restrict__ wA,
                                                 const __hip_bfloat16* __restrict__ wB, const __hip_bfloat16* __restrict__ woA,
                                                 const __hip_bfloat16* __restrict__ woB, const __hip_bfloat16* __restrict__ gw,
                                                 char* __restrict__ ws) {
    extern __shared__ __attribute__((aligned(16))) char lds[];
    const int tid = threadIdx.x, lane = tid & 63;
    const int wid = __builtin_amdgcn_readfirstlane(tid >> 6);
    const int b = blockIdx.x;
    const int hg = b & 7, br = (b >> 3) & 1, rb = b >> 4;
    const char* xg = (const char*)x + (size_t)rb * 128 * 1024;
    const char* wg = (const char*)(br ? wB : wA) + hg * 128;
    const unsigned lds0 = (unsigned)(uintptr_t)lds;

    const int pr = lane >> 3, pc = lane & 7;
    const char* xsrc[2]; const char* wsrc[3];
#pragma unroll
    for (int i = 0; i < 2; ++i) { const int row = (wid * 2 + i) * 8 + pr; xsrc[i] = xg + row * 1024 + ((pc ^ ((row >> 1) & 7)) << 4); }
#pragma unroll
    for (int i = 0; i < 3; ++i) {
        const int k = wid * 8 + pr; const int f = ((k >> 1) & 1) | (((k >> 3) & 1) << 1);
        wsrc[i] = wg + (size_t)k * 3072 + i * 1024 + (((pc >> 1) ^ f) << 5) + ((pc & 1) << 4);
    }
    auto stage = [&](int t) {
        const unsigned base = lds0 + (t & (K1_NST - 1)) * K1_STAGE;
#pragma unroll
        for (int i = 0; i < 2; ++i) glds16(xsrc[i] + t * 128, base + (wid * 2 + i) * 1024);
#pragma unroll
        for (int i = 0; i < 3; ++i) glds16(wsrc[i] + (size_t)t * 64 * 3072, base + K1_XB + i * 8192 + wid * 1024);
    };
    stage(0); stage(1); stage(2);

    const int wm = wid & 1, hl = wid >> 1;
    const int fr = lane & 15, fg = lane >> 4;
    int xoff[2];
#pragma unroll
    for (int kk = 0; kk < 2; ++kk) xoff[kk] = (wm * 64 + fr) * 128 + (((kk * 4 + fg) ^ ((fr >> 1) & 7)) << 4);
    int woff;
    { const int k = 8 * fg + (fr >> 2); const int f = ((k >> 1) & 1) | (((k >> 3) & 1) << 1);
      woff = K1_XB + k * 128 + ((hl ^ f) << 5) + ((fr & 3) << 3); }
    f32x4 acc[3][4];
#pragma unroll
    for (int s = 0; s < 3; ++s)
#pragma unroll
        for (int m = 0; m < 4; ++m) acc[s][m] = f32x4{0.f, 0.f, 0.f, 0.f};

#pragma unroll
    for (int t = 0; t < 8; ++t) {
        if (t < 6) asm volatile("s_waitcnt vmcnt(10)\n\ts_barrier" ::: "memory");
        else if (t == 6) asm volatile("s_waitcnt vmcnt(5)\n\ts_barrier" ::: "memory");
        else asm volatile("s_waitcnt vmcnt(0)\n\ts_barrier" ::: "memory");
        if (t + 3 < 8) stage(t + 3);
        const lds_cptr sb = (lds_cptr)lds + (t & (K1_NST - 1)) * K1_STAGE;
#pragma unroll
        for (int kk = 0; kk < 2; ++kk) {
            bf16x8 xf[4], wf[3];
#pragma unroll
            for (int m = 0; m < 4; ++m) xf[m] = lds_ld16(sb + xoff[kk] + m * 2048);
#pragma unroll
            for (int s = 0; s < 3; ++s) {
                const s16x4 lo = lds_tr(sb + woff + s * 8192 + kk * 4096), hi = lds_tr(sb + woff + s * 8192 + kk * 4096 + 512);
                wf[s] = __builtin_bit_cast(bf16x8, __builtin_shufflevector(lo, hi, 0, 1, 2, 3, 4, 5, 6, 7));
            }
#pragma unroll
            for (int m = 0; m < 4; ++m) {
                acc[0][m] = __builtin_amdgcn_mfma_f32_16x16x32_bf16(wf[0], xf[m], acc[0][m], 0, 0, 0);
                acc[1][m] = __builtin_amdgcn_mfma_f32_16x16x32_bf16(wf[1], xf[m], acc[1][m], 0, 0, 0);
                acc[2][m] = __builtin_amdgcn_mfma_f32_16x16x32_bf16(xf[m], wf[2], acc[2][m], 0, 0, 0);
            }
        }
    }
    const int head = hg * 4 + hl;
    const size_t hb = ((size_t)br * NH + head) * 65536;
#pragma unroll
    for (int m = 0; m < 4; ++m) {
        const int s16 = rb * 8 + wm * 4 + m;
        const size_t qk = hb + (s16 >> 1) * 1024 + (fg >> 1) * 512 + ((s16 & 1) * 16 + fr) * 16 + (fg & 1) * 8;
        *(u32x2*)(ws + WS_Q + qk) = u32x2{cvtpk(acc[0][m][0] * QSCALE, acc[0][m][1] * QSCALE), cvtpk(acc[0][m][2] * QSCALE, acc[0][m][3] * QSCALE)};
        *(u32x2*)(ws + WS_K + qk) = u32x2{cvtpk(acc[1][m][0], acc[1][m][1]), cvtpk(acc[1][m][2], acc[1][m][3])};
        const size_t vo = hb + s16 * 512 + (fg >> 1) * 256 + fr * 16 + (fg & 1) * 8;
        *(u32x2*)(ws + WS_V + vo) = u32x2{cvtpk(acc[2][m][0], acc[2][m][1]), cvtpk(acc[2][m][2], acc[2][m][3])};
    }
    {
        const int row = b * 8 + wid;
        const u32x4 xv = *(const u32x4*)((const char*)x + row * 1024 + lane * 16);
        const u32x4 gv = *(const u32x4*)((const char*)gw + lane * 16);
        float z = 0.f;
#pragma unroll
        for (int i = 0; i < 4; ++i) {
            const unsigned p = cvtpk(bflo(xv[i]) * bflo(gv[i]), bfhi(xv[i]) * bfhi(gv[i]));
            z += bflo(p) + bfhi(p);
        }
#pragma unroll
        for (int o = 32; o > 0; o >>= 1) z += __shfl_xor(z, o);
        z = bf16r(z);
        const float g = bf16r(1.f / (1.f + __expf(-z)));
        if (lane == 0) ((float*)(ws + WS_G))[row] = g;
    }
    if (tid < 256) {
        const int ks = b >> 2, n = (b & 3) * 128 + (tid & 127), hh = tid >> 7;
        const unsigned short* src = (const unsigned short*)(ks < 32 ? woA : woB) + (size_t)((ks & 31) * 16 + hh * 8) * DM + n;
        unsigned v[8];
#pragma unroll
        for (int j = 0; j < 8; ++j) v[j] = src[j * DM];
        *(u32x4*)(ws + WS_W + ((size_t)ks * 16 + (n >> 5)) * 1024 + hh * 512 + (n & 31) * 16) =
            u32x4{v[0] | (v[1] << 16), v[2] | (v[3] << 16), v[4] | (v[5] << 16), v[6] | (v[7] << 16)};
    }
}

constexpr int K2_V = 65536, K2_ONES = 131072, K2_LDS = K2_ONES + 4096;
#define SMF(a, plo, acc) __builtin_amdgcn_smfmac_f32_32x32x32_bf16(a, __builtin_bit_cast(bf16x16, plo), acc, 0xDDDD, 0, 0)
#define LO8(p) __builtin_shufflevector(p, p, 0, 1, 2, 3, 4, 5, 6, 7)
#define HI8(p) __builtin_shufflevector(p, p, 8, 9, 10, 11, 12, 13, 14, 15)
#define MF32(a, b, c) __builtin_amdgcn_mfma_f32_32x32x16_bf16(a, b, c, 0, 0, 0)

__device__ __forceinline__ float halfmax(float m) {
    auto rr = __builtin_amdgcn_permlane32_swap(__float_as_uint(m), __float_as_uint(m), false, false);
    return fmaxf(__uint_as_float(rr[0]), __uint_as_float(rr[1]));
}
__device__ __forceinline__ void store_o(char* dst, const f32x16& o, float sc) {
    unsigned a0 = cvtpk(o[0] * sc, o[1] * sc), a1 = cvtpk(o[2] * sc, o[3] * sc);
    unsigned b0 = cvtpk(o[4] * sc, o[5] * sc), b1 = cvtpk(o[6] * sc, o[7] * sc);
    auto r0 = __builtin_amdgcn_permlane32_swap(a0, b0, false, false);
    auto r1 = __builtin_amdgcn_permlane32_swap(a1, b1, false, false);
    *(u32x4*)dst = u32x4{r0[0], r1[0], r0[1], r1[1]};
}

__global__ __launch_bounds__(512, 2) void k2_attn(char* __restrict__ ws) {
    extern __shared__ __attribute__((aligned(16))) char lds[];
    const int tid = threadIdx.x, lane = tid & 63, r32 = lane & 31, hh = lane >> 5;
    const int wid = __builtin_amdgcn_readfirstlane(tid >> 6);
    const int b = blockIdx.x;
    const int head = (b & 7) * 4 + ((b >> 3) & 3), qb = b >> 5;
    const int qt = qb * 8 + wid;
    const unsigned lds0 = (unsigned)(uintptr_t)lds;
    const size_t hA = (size_t)head * 65536, hB = (size_t)(NH + head) * 65536;

    u32x4 qB = gload16_asm(ws + WS_Q + hB + qt * 1024 + lane * 16);
    const char* ksrc = ws + WS_K + hB + wid * 1024 + lane * 16;
    const char* vsrc = ws + WS_V + hB + wid * 1024 + lane * 16;
    glds16(ksrc, lds0 + wid * 1024); glds16(vsrc, lds0 + K2_V + wid * 1024);
    const int qp = qt > 0 ? qt - 1 : 0;
    const int vl = hh * 256 + (r32 & 15) * 16;
    u32x4 qA = gload16_asm(ws + WS_Q + hA + qt * 1024 + lane * 16);
    u32x4 kA0 = gload16_asm(ws + WS_K + hA + qp * 1024 + lane * 16);
    u32x4 kA1 = gload16_asm(ws + WS_K + hA + qt * 1024 + lane * 16);
    u32x4 vA0 = gload16_asm(ws + WS_V + hA + qp * 1024 + 512 + vl);
    u32x4 vA1 = gload16_asm(ws + WS_V + hA + qt * 1024 + vl);
    u32x4 vA2 = gload16_asm(ws + WS_V + hA + qt * 1024 + 512 + vl);
    float gate; asm volatile("global_load_dword %0, %1, off" : "=v"(gate) : "v"(ws + WS_G + (qt * 32 + r32) * 4) : "memory");
#pragma unroll
    for (int c = 1; c < 8; ++c) { glds16(ksrc + c * 8192, lds0 + c * 8192 + wid * 1024); glds16(vsrc + c * 8192, lds0 + K2_V + c * 8192 + wid * 1024); }
    *(__attribute__((address_space(3))) u32x2*)((lds_ptr)lds + K2_ONES + tid * 8) = u32x2{0x3f803f80u, 0x3f803f80u};

    asm volatile("s_waitcnt vmcnt(21) lgkmcnt(0)\n\ts_barrier" : "+v"(qB) :: "memory");
    const bf16x8 qf = __builtin_bit_cast(bf16x8, qB);
    lds_cptr kp = (lds_cptr)lds + lane * 16;
    lds_cptr vp = (lds_cptr)lds + (r32 < 16 ? K2_V : K2_ONES) + vl;
    const int vstep = r32 < 16 ? 4096 : 0;

    f32x16 negm;
    { f32x16 z = {}; f32x16 s = MF32(lds_ld16(kp), qf, z);
      float m = fmaxf(fmaxf(s[0], s[1]), s[2]);
#pragma unroll
      for (int r = 3; r < 15; r += 2) m = fmaxf(fmaxf(m, s[r]), s[r + 1]);
      m = halfmax(fmaxf(m, s[15]));
#pragma unroll
      for (int r = 0; r < 16; ++r) negm[r] = -m; }
    PIN(negm);
    f32x16 o = {};
    f32x16 sA = MF32(lds_ld16(kp), qf, negm), sB;
#define EXP16(s) _Pragma("unroll") for (int r = 0; r < 16; ++r) s[r] = __builtin_amdgcn_exp2f(s[r])
#define STEP(cur, nxt, u, last) do { \
        const bf16x8 v0 = lds_ld16(vp + (u) * 1024), v1 = lds_ld16(vp + (u) * 1024 + 512); \
        if (!(last)) nxt = MF32(lds_ld16(kp + ((u) + 1) * 1024), qf, negm); \
        EXP16(cur); \
        o = SMF(v0, LO8(cur), o); o = SMF(v1, HI8(cur), o); } while (0)
    for (int t = 0; t < 64; t += 4) {
        if (t == 4) asm volatile("s_waitcnt vmcnt(12)\n\ts_barrier" ::: "memory");
        if (t == 12) asm volatile("s_waitcnt vmcnt(0)\n\ts_barrier" ::: "memory");
        STEP(sA, sB, 0, false); STEP(sB, sA, 1, false); STEP(sA, sB, 2, false); STEP(sB, sA, 3, t == 60);
        kp += 4096; vp += vstep;
    }
    {
        float chk = o[8];
#pragma unroll
        for (int r = 0; r < 8; ++r) chk = fmaxf(chk, fabsf(o[r]));
        const bool bad = !(chk < 1e30f) || !(o[0] + o[1] + o[2] + o[3] + o[4] + o[5] + o[6] + o[7] + o[8] < 1e37f);
        if (__any(bad)) {
            kp = (lds_cptr)lds + lane * 16;
            vp = (lds_cptr)lds + (r32 < 16 ? K2_V : K2_ONES) + vl;
            const int vs1 = r32 < 16 ? 1024 : 0;
            float m = -INFINITY; o = f32x16{};
#pragma unroll 1
            for (int t = 0; t < 64; ++t) {
                f32x16 z = {}; f32x16 s = MF32(lds_ld16(kp), qf, z);
                float tm = s[0];
#pragma unroll
                for (int r = 1; r < 16; ++r) tm = fmaxf(tm, s[r]);
                const float mn = fmaxf(m, halfmax(tm));
                const float al = __builtin_amdgcn_exp2f(m - mn); m = mn;
#pragma unroll
                for (int r = 0; r < 16; ++r) { s[r] = __builtin_amdgcn_exp2f(s[r] - mn); o[r] *= al; }
                o = SMF(lds_ld16(vp), LO8(s), o); o = SMF(lds_ld16(vp + 512), HI8(s), o);
                kp += 1024; vp += vs1;
            }
        }
    }
    asm volatile("" : "+v"(qA), "+v"(kA0), "+v"(kA1), "+v"(vA0), "+v"(vA1), "+v"(vA2), "+v"(gate));
    char* od = ws + WS_O + (size_t)qt * 65536 + lane * 16;
    store_o(od + (NH + head) * 1024, o, bf16r(1.f - gate) * __builtin_amdgcn_rcpf(o[8]));

    {
        const f32x16 z = {};
        const bf16x8 qa = __builtin_bit_cast(bf16x8, qA);
        f32x16 s0 = MF32(__builtin_bit_cast(bf16x8, kA0), qa, z), s1 = MF32(__builtin_bit_cast(bf16x8, kA1), qa, z);
        float m = -INFINITY;
#pragma unroll
        for (int r = 0; r < 16; ++r) {
            const int d1 = r32 - crow(r, hh);
            s1[r] = (d1 >= 0 && d1 <= 14 && !(d1 & 1)) ? s1[r] : -INFINITY; m = fmaxf(m, s1[r]);
            if (r >= 8) { const int d0 = d1 + 32; s0[r] = (qt > 0 && d0 <= 14 && !(d0 & 1)) ? s0[r] : -INFINITY; m = fmaxf(m, s0[r]); }
        }
        m = halfmax(m);
#pragma unroll
        for (int r = 0; r < 16; ++r) { s1[r] = __builtin_amdgcn_exp2f(s1[r] - m); if (r >= 8) s0[r] = __builtin_amdgcn_exp2f(s0[r] - m); }
        const u32x4 ones = {0x3f803f80u, 0x3f803f80u, 0x3f803f80u, 0x3f803f80u};
        if (r32 >= 16) { vA0 = ones; vA1 = ones; vA2 = ones; }
        f32x16 oa = {};
        oa = SMF(__builtin_bit_cast(bf16x8, vA0), HI8(s0), oa);
        oa = SMF(__builtin_bit_cast(bf16x8, vA1), LO8(s1), oa);
        oa = SMF(__builtin_bit_cast(bf16x8, vA2), HI8(s1), oa);
        store_o(od + head * 1024, oa, gate * __builtin_amdgcn_rcpf(oa[8]));
    }
}

__global__ __launch_bounds__(512, 2) void k3_out(const char* __restrict__ ws, __hip_bfloat16* __restrict__ out) {
    extern __shared__ __attribute__((aligned(16))) char lds[];
    const int tid = threadIdx.x, lane = tid & 63, r32 = lane & 31, hh = lane >> 5;
    const int wid = __builtin_amdgcn_readfirstlane(tid >> 6);
    const int b = blockIdx.x;
    const int rb = (b & 7) * 4 + ((b >> 3) & 3), nb = b >> 5;
    const char* osrc = ws + WS_O + (size_t)rb * 2 * 65536 + wid * 8192 + lane * 16;
    const char* wsrc = ws + WS_W + (size_t)wid * 8 * 16384 + nb * 2048 + lane * 16;
    bf16x8 of[2][8], wf[2][8];
#pragma unroll
    for (int k = 0; k < 8; ++k)
#pragma unroll
        for (int i = 0; i < 2; ++i) {
            of[i][k] = *(const bf16x8*)(osrc + i * 65536 + k * 1024);
            wf[i][k] = *(const bf16x8*)(wsrc + k * 16384 + i * 1024);
        }
    f32x16 acc[2][2] = {};
#pragma unroll
    for (int k = 0; k < 8; ++k)
#pragma unroll
        for (int m = 0; m < 2; ++m)
#pragma unroll
            for (int n = 0; n < 2; ++n) acc[m][n] = MF32(wf[n][k], of[m][k], acc[m][n]);
    float* red = (float*)lds;
#pragma unroll
    for (int m = 0; m < 2; ++m)
#pragma unroll
        for (int n = 0; n < 2; ++n)
#pragma unroll
            for (int q = 0; q < 4; ++q)
                *(f32x4*)(red + wid * 4096 + ((m * 2 + n) * 4 + q) * 256 + lane * 4) =
                    f32x4{acc[m][n][4 * q], acc[m][n][4 * q + 1], acc[m][n][4 * q + 2], acc[m][n][4 * q + 3]};
    __syncthreads();
    f32x4 s0 = {}, s1 = {};
#pragma unroll
    for (int p = 0; p < 8; ++p) {
        s0 += *(const f32x4*)(red + p * 4096 + (2 * wid) * 256 + lane * 4);
        s1 += *(const f32x4*)(red + p * 4096 + (2 * wid + 1) * 256 + lane * 4);
    }
    unsigned a0 = cvtpk(s0[0], s0[1]), a1 = cvtpk(s0[2], s0[3]), b0 = cvtpk(s1[0], s1[1]), b1 = cvtpk(s1[2], s1[3]);
    auto r0 = __builtin_amdgcn_permlane32_swap(a0, b0, false, false);
    auto r1 = __builtin_amdgcn_permlane32_swap(a1, b1, false, false);
    const int tile = wid >> 1, m = tile >> 1, n = tile & 1;
    const int row = rb * 64 + m * 32 + r32, col = nb * 64 + n * 32 + (wid & 1) * 16 + hh * 8;
    *(u32x4*)((char*)out + (size_t)row * 1024 + col * 2) = u32x4{r0[0], r1[0], r0[1], r1[1]};
}

extern "C" void kernel_launch(void* const* d_in, const int* in_sizes, int n_in, void* d_out, int out_size, void* d_ws,
                              size_t ws_size, hipStream_t stream) {
    const __hip_bfloat16* x = (const __hip_bfloat16*)d_in[0];
    const __hip_bfloat16* wA = (const __hip_bfloat16*)d_in[1];
    const __hip_bfloat16* woA = (const __hip_bfloat16*)d_in[2];
    const __hip_bfloat16* wB = (const __hip_bfloat16*)d_in[4];
    const __hip_bfloat16* woB = (const __hip_bfloat16*)d_in[5];
    const __hip_bfloat16* gw = (const __hip_bfloat16*)d_in[6];
    (void)hipFuncSetAttribute((const void*)k1_qkv, hipFuncAttributeMaxDynamicSharedMemorySize, K1_STAGE * K1_NST);
    (void)hipFuncSetAttribute((const void*)k2_attn, hipFuncAttributeMaxDynamicSharedMemorySize, K2_LDS);
    (void)hipFuncSetAttribute((const void*)k3_out, hipFuncAttributeMaxDynamicSharedMemorySize, 131072);
    k1_qkv<<<256, 512, K1_STAGE * K1_NST, stream>>>(x, wA, wB, woA, woB, gw, (char*)d_ws);
    k2_attn<<<256, 512, K2_LDS, stream>>>((char*)d_ws);
    k3_out<<<256, 512, 131072, stream>>>((const char*)d_ws, (__hip_bfloat16*)d_out);
}
```

```cpp
#include <hip/hip_runtime.h>
#include <hip/hip_bf16.h>
#include <stdint.h>

typedef __attribute__((ext_vector_type(8))) __bf16 bf16x8;
typedef __attribute__((ext_vector_type(16))) __bf16 bf16x16;
typedef __attribute__((ext_vector_type(4))) short s16x4;
typedef __attribute__((ext_vector_type(4))) float f32x4;
typedef __attribute__((ext_vector_type(8))) float f32x8;
typedef __attribute__((ext_vector_type(16))) float f32x16;
typedef __attribute__((ext_vector_type(2))) unsigned u32x2;
typedef __attribute__((ext_vector_type(4))) unsigned u32x4;
typedef __attribute__((address_space(3))) const char* lds_cptr;
typedef __attribute__((address_space(3))) char* lds_ptr;

constexpr int SEQ = 2048, DM = 512, NH = 32;
constexpr float QSCALE = 0.25f * 1.4426950408889634f;

constexpr size_t WS_Q = 0;
constexpr size_t WS_K = WS_Q + (4u << 20);
constexpr size_t WS_V = WS_K + (4u << 20);
constexpr size_t WS_O = WS_V + (4u << 20);
constexpr size_t WS_W = WS_O + (4u << 20);
constexpr size_t WS_G = WS_W + (1u << 20);

#define STAMP_DECL do {} while (0)
#define STAMP(i) do {} while (0)
#define STAMP_FLUSH(ws, kid) do {} while (0)
#define PIN(x) asm volatile("" : "+v"(x))
#define SBAR() __builtin_amdgcn_sched_barrier(0)

__device__ __forceinline__ unsigned cvtpk(float lo, float hi) {
    typedef __attribute__((ext_vector_type(2))) float f32x2_; typedef __attribute__((ext_vector_type(2))) __bf16 bf16x2_;
    return __builtin_bit_cast(unsigned, __builtin_convertvector(f32x2_{lo, hi}, bf16x2_));
}
__device__ __forceinline__ float bf16r(float x) { return __uint_as_float(cvtpk(x, x) << 16); }
__device__ __forceinline__ float bflo(unsigned u) { return __uint_as_float(u << 16); }
__device__ __forceinline__ float bfhi(unsigned u) { return __uint_as_float(u & 0xffff0000u); }
__device__ __forceinline__ void glds16(const void* g, unsigned lds_base) {
    unsigned sv;
    asm volatile("s_mov_b32 %0, m0\n\ts_mov_b32 m0, %2\n\ts_nop 0\n\tglobal_load_lds_dwordx4 %1, off\n\ts_mov_b32 m0, %0"
                 : "=&s"(sv) : "v"(g), "s"(lds_base) : "memory");
}
__device__ __forceinline__ void glds16s(const void* sbase, unsigned voff, unsigned lds_base) {
    unsigned sv;
    asm volatile("s_mov_b32 %0, m0\n\ts_mov_b32 m0, %3\n\ts_nop 0\n\tglobal_load_lds_dwordx4 %1, %2\n\ts_mov_b32 m0, %0"
                 : "=&s"(sv) : "v"(voff), "s"(sbase), "s"(lds_base) : "memory");
}
__device__ __forceinline__ u32x4 gload16_asm(const void* g) {
    u32x4 r; asm volatile("global_load_dwordx4 %0, %1, off" : "=v"(r) : "v"(g) : "memory"); return r;
}
#define ST_MODE 2
__device__ __forceinline__ void st8(void* p, u32x2 v) {
    asm volatile("global_store_dwordx2 %0, %1, off sc1" :: "v"(p), "v"(v) : "memory");
}
__device__ __forceinline__ bf16x8 lds_ld16(lds_cptr p) { return *(const __attribute__((address_space(3))) bf16x8*)p; }
__device__ __forceinline__ s16x4 lds_tr(lds_cptr p) {
    return __builtin_amdgcn_ds_read_tr16_b64_v4i16((__attribute__((address_space(3))) s16x4*)p);
}
__device__ __forceinline__ int crow(int r, int hi) { return (r & 3) + 8 * (r >> 2) + 4 * hi; }

constexpr int K1_STAGE = 40960, K1_XB = 16384, K1_NST = 4;
__global__ __launch_bounds__(512, 2) void k1_qkv(const __hip_bfloat16* __restrict__ x, const __hip_bfloat16* __restrict__ wA,
                                                 const __hip_bfloat16* __restrict__ wB, const __hip_bfloat16* __restrict__ woA,
                                                 const __hip_bfloat16* __restrict__ woB, const __hip_bfloat16* __restrict__ gw,
                                                 char* __restrict__ ws) {
    extern __shared__ __attribute__((aligned(16))) char lds[];
    const int tid = threadIdx.x, lane = tid & 63;
    const int wid = __builtin_amdgcn_readfirstlane(tid >> 6);
    const int b = blockIdx.x;
    STAMP_DECL; STAMP(0);
    const int hg = b & 7, br = (b >> 3) & 1, rb = b >> 4;
    const char* xg = (const char*)x + (size_t)rb * 128 * 1024;
    const char* wg = (const char*)(br ? wB : wA) + hg * 128;
    const unsigned lds0 = (unsigned)(uintptr_t)lds;

    const int pr = lane >> 3, pc = lane & 7;
    const char* xsrc[2]; const char* wsrc[3];
#pragma unroll
    for (int i = 0; i < 2; ++i) { const int row = (wid * 2 + i) * 8 + pr; xsrc[i] = xg + row * 1024 + ((pc ^ ((row >> 1) & 7)) << 4); }
#pragma unroll
    for (int i = 0; i < 3; ++i) {
        const int k = wid * 8 + pr; const int f = ((k >> 1) & 1) | (((k >> 3) & 1) << 1);
        wsrc[i] = wg + (size_t)k * 3072 + i * 1024 + (((pc >> 1) ^ f) << 5) + ((pc & 1) << 4);
    }
    auto stage = [&](int t) {
        const unsigned base = lds0 + (t & (K1_NST - 1)) * K1_STAGE;
#pragma unroll
        for (int i = 0; i < 2; ++i) glds16(xsrc[i] + t * 128, base + (wid * 2 + i) * 1024);
#pragma unroll
        for (int i = 0; i < 3; ++i) glds16(wsrc[i] + (size_t)t * 64 * 3072, base + K1_XB + i * 8192 + wid * 1024);
    };
    stage(0); stage(1); stage(2);
    STAMP(1);

    const int wm = wid & 1, hl = wid >> 1;
    const int fr = lane & 15, fg = lane >> 4;
    int xoff[2];
#pragma unroll
    for (int kk = 0; kk < 2; ++kk) xoff[kk] = (wm * 64 + fr) * 128 + (((kk * 4 + fg) ^ ((fr >> 1) & 7)) << 4);
    int woff;
    { const int k = 8 * fg + (fr >> 2); const int f = ((k >> 1) & 1) | (((k >> 3) & 1) << 1);
      woff = K1_XB + k * 128 + ((hl ^ f) << 5) + ((fr & 3) << 3); }
    f32x4 acc[3][4];
#pragma unroll
    for (int s = 0; s < 3; ++s)
#pragma unroll
        for (int m = 0; m < 4; ++m) acc[s][m] = f32x4{0.f, 0.f, 0.f, 0.f};

#pragma unroll
    for (int t = 0; t < 8; ++t) {
        if (t < 6) asm volatile("s_waitcnt vmcnt(10)\n\ts_barrier" ::: "memory");
        else if (t == 6) asm volatile("s_waitcnt vmcnt(5)\n\ts_barrier" ::: "memory");
        else asm volatile("s_waitcnt vmcnt(0)\n\ts_barrier" ::: "memory");
        if (t == 0) STAMP(2); if (t == 1) STAMP(3); if (t == 2) STAMP(4); if (t == 4) STAMP(5); if (t == 7) STAMP(6);
        if (t + 3 < 8) stage(t + 3);
        const lds_cptr sb = (lds_cptr)lds + (t & (K1_NST - 1)) * K1_STAGE;
#pragma unroll
        for (int kk = 0; kk < 2; ++kk) {
            bf16x8 xf[4], wf[3];
#pragma unroll
            for (int m = 0; m < 4; ++m) xf[m] = lds_ld16(sb + xoff[kk] + m * 2048);
#pragma unroll
            for (int s = 0; s < 3; ++s) {
                const s16x4 lo = lds_tr(sb + woff + s * 8192 + kk * 4096), hi = lds_tr(sb + woff + s * 8192 + kk * 4096 + 512);
                wf[s] = __builtin_bit_cast(bf16x8, __builtin_shufflevector(lo, hi, 0, 1, 2, 3, 4, 5, 6, 7));
            }
#pragma unroll
            for (int m = 0; m < 4; ++m) {
                acc[0][m] = __builtin_amdgcn_mfma_f32_16x16x32_bf16(wf[0], xf[m], acc[0][m], 0, 0, 0);
                acc[1][m] = __builtin_amdgcn_mfma_f32_16x16x32_bf16(wf[1], xf[m], acc[1][m], 0, 0, 0);
                acc[2][m] = __builtin_amdgcn_mfma_f32_16x16x32_bf16(xf[m], wf[2], acc[2][m], 0, 0, 0);
            }
        }
    }
    STAMP(7);
    const int head = hg * 4 + hl;
    const size_t hb = ((size_t)br * NH + head) * 65536;
#pragma unroll
    for (int m = 0; m < 4; ++m) {
        const int s16 = rb * 8 + wm * 4 + m;
        const size_t qk = hb + (s16 >> 1) * 1024 + (fg >> 1) * 512 + ((s16 & 1) * 16 + fr) * 16 + (fg & 1) * 8;
        st8(ws + WS_Q + qk, u32x2{cvtpk(acc[0][m][0] * QSCALE, acc[0][m][1] * QSCALE), cvtpk(acc[0][m][2] * QSCALE, acc[0][m][3] * QSCALE)});
        st8(ws + WS_K + qk, u32x2{cvtpk(acc[1][m][0], acc[1][m][1]), cvtpk(acc[1][m][2], acc[1][m][3])});
        const size_t vo = hb + s16 * 512 + (fg >> 1) * 256 + fr * 16 + (fg & 1) * 8;
        st8(ws + WS_V + vo, u32x2{cvtpk(acc[2][m][0], acc[2][m][1]), cvtpk(acc[2][m][2], acc[2][m][3])});
    }
    STAMP(8);
    {
        const int row = b * 8 + wid;
        const u32x4 xv = *(const u32x4*)((const char*)x + row * 1024 + lane * 16);
        const u32x4 gv = *(const u32x4*)((const char*)gw + lane * 16);
        float z = 0.f;
#pragma unroll
        for (int i = 0; i < 4; ++i) {
            const unsigned p = cvtpk(bflo(xv[i]) * bflo(gv[i]), bfhi(xv[i]) * bfhi(gv[i]));
            z += bflo(p) + bfhi(p);
        }
#pragma unroll
        for (int o = 32; o > 0; o >>= 1) z += __shfl_xor(z, o);
        z = bf16r(z);
        const float g = bf16r(1.f / (1.f + __expf(-z)));
        if (lane == 0) ((float*)(ws + WS_G))[row] = g;
    }
    if (tid < 256) {
        const int ks = b >> 2, n = (b & 3) * 128 + (tid & 127), hh = tid >> 7;
        const unsigned short* src = (const unsigned short*)(ks < 32 ? woA : woB) + (size_t)((ks & 31) * 16 + hh * 8) * DM + n;
        unsigned v[8];
#pragma unroll
        for (int j = 0; j < 8; ++j) v[j] = src[j * DM];
        *(u32x4*)(ws + WS_W + ((size_t)ks * 16 + (n >> 5)) * 1024 + hh * 512 + (n & 31) * 16) =
            u32x4{v[0] | (v[1] << 16), v[2] | (v[3] << 16), v[4] | (v[5] << 16), v[6] | (v[7] << 16)};
    }
    STAMP(9); STAMP_FLUSH(ws, 0);
}

constexpr int K2_V = 65536, K2_ONES = 131072, K2_LDS = K2_ONES + 9216;
#define SMF(a, plo, acc) __builtin_amdgcn_smfmac_f32_32x32x32_bf16(a, __builtin_bit_cast(bf16x16, plo), acc, 0xDDDD, 0, 0)
#define LO8(p) __builtin_shufflevector(p, p, 0, 1, 2, 3, 4, 5, 6, 7)
#define HI8(p) __builtin_shufflevector(p, p, 8, 9, 10, 11, 12, 13, 14, 15)
#define MF32(a, b, c) __builtin_amdgcn_mfma_f32_32x32x16_bf16(a, b, c, 0, 0, 0)

__device__ __forceinline__ float halfmax(float m) {
    auto rr = __builtin_amdgcn_permlane32_swap(__float_as_uint(m), __float_as_uint(m), false, false);
    return fmaxf(__uint_as_float(rr[0]), __uint_as_float(rr[1]));
}
__device__ __forceinline__ void store_o(char* dst, const f32x16& o, float sc) {
    unsigned a0 = cvtpk(o[0] * sc, o[1] * sc), a1 = cvtpk(o[2] * sc, o[3] * sc);
    unsigned b0 = cvtpk(o[4] * sc, o[5] * sc), b1 = cvtpk(o[6] * sc, o[7] * sc);
    auto r0 = __builtin_amdgcn_permlane32_swap(a0, b0, false, false);
    auto r1 = __builtin_amdgcn_permlane32_swap(a1, b1, false, false);
    *(u32x4*)dst = u32x4{r0[0], r1[0], r0[1], r1[1]};
}

__global__ __launch_bounds__(512, 2) void k2_attn(char* __restrict__ ws) {
    extern __shared__ __attribute__((aligned(16))) char lds[];
    const int tid = threadIdx.x, lane = tid & 63, r32 = lane & 31, hh = lane >> 5;
    const int wid = __builtin_amdgcn_readfirstlane(tid >> 6);
    const int b = blockIdx.x;
    STAMP_DECL; STAMP(0);
    const int head = (b & 7) * 4 + ((b >> 3) & 3), qb = b >> 5;
    const int qt = qb * 8 + wid;
    const unsigned lds0 = (unsigned)(uintptr_t)lds;
    const size_t hA = (size_t)head * 65536, hB = (size_t)(NH + head) * 65536;

    u32x4 qB = gload16_asm(ws + WS_Q + hB + qt * 1024 + lane * 16);
    const char* ksrc = ws + WS_K + hB + wid * 1024;
    const char* vsrc = ws + WS_V + hB + wid * 1024;
    unsigned l16 = lane * 16; const unsigned ldsw = lds0 + wid * 1024;
#define DMA_CHUNK(c) do { glds16s(ksrc + (c) * 8192, l16, ldsw + (c) * 8192); glds16s(vsrc + (c) * 8192, l16, ldsw + K2_V + (c) * 8192); } while (0)
    DMA_CHUNK(0); DMA_CHUNK(1); DMA_CHUNK(2);
    const int qp = qt > 0 ? qt - 1 : 0;
    const int vl = hh * 256 + (r32 & 15) * 16;
    if (tid < 64) *(__attribute__((address_space(3))) u32x4*)((lds_ptr)lds + K2_ONES + 8192 + tid * 16) = u32x4{0x3f803f80u, 0x3f803f80u, 0x3f803f80u, 0x3f803f80u};
    *(__attribute__((address_space(3))) u32x4*)((lds_ptr)lds + K2_ONES + tid * 16) = u32x4{0x3f803f80u, 0x3f803f80u, 0x3f803f80u, 0x3f803f80u};

    STAMP(1);
    asm volatile("s_waitcnt vmcnt(4) lgkmcnt(0)\n\ts_barrier" : "+v"(qB) :: "memory");
    STAMP(2);
    const bf16x8 qf = __builtin_bit_cast(bf16x8, qB);
    lds_cptr kp = (lds_cptr)lds + lane * 16;
    lds_cptr vp = (lds_cptr)lds + (r32 < 16 ? K2_V : K2_ONES) + vl;
    const int vstep = r32 < 16 ? 8192 : 0;

    float negm;
    { f32x16 z = {}; f32x16 s = MF32(lds_ld16(kp), qf, z);
      float m = fmaxf(fmaxf(s[0], s[1]), s[2]);
#pragma unroll
      for (int r = 3; r < 15; r += 2) m = fmaxf(fmaxf(m, s[r]), s[r + 1]);
      negm = -halfmax(fmaxf(m, s[15])); }
    f32x16 o;
    {
        float o0, o1, o2, o3, o4, o5, o6, o7, o8;
        unsigned kpa = (unsigned)(uintptr_t)kp, vpa = (unsigned)(uintptr_t)vp;
        const char* kb = ksrc + 3 * 8192; const char* vb = vsrc + 3 * 8192; unsigned ldst = ldsw + 3 * 8192, cnt = 0;
#define XE(b, i) "v_exp_f32 v[" #b "+" #i "], v[" #b "+" #i "]\n\t"
#define ASTEP(cur, nxt, u) \
        XE(cur, 0) XE(cur, 1) \
        "s_waitcnt lgkmcnt(2)\n\t" \
        "v_smfmac_f32_32x32x32_bf16 v[96:111], v[140:143], v[" #nxt "+8:" #nxt "+15], v144\n\t" \
        "ds_read_b128 v[140:143], %[vp] offset:" #u "*1024+512\n\t" \
        XE(cur, 2) XE(cur, 3) XE(cur, 4) XE(cur, 5) XE(cur, 6) \
        "s_waitcnt lgkmcnt(2)\n\t" \
        "v_mfma_f32_32x32x16_bf16 v[" #nxt ":" #nxt "+15], v[132:135], %[q], v[112:127]\n\t" \
        "ds_read_b128 v[132:135], %[kp] offset:" #u "*1024+2048\n\t" \
        XE(cur, 7) XE(cur, 8) XE(cur, 9) XE(cur, 10) XE(cur, 11) \
        "s_waitcnt lgkmcnt(2)\n\t" \
        "v_smfmac_f32_32x32x32_bf16 v[96:111], v[136:139], v[" #cur ":" #cur "+7], v144\n\t" \
        "ds_read_b128 v[136:139], %[vp] offset:" #u "*1024+1024\n\t" \
        XE(cur, 12) XE(cur, 13) XE(cur, 14) XE(cur, 15)
#define XM(r, v) "v_mov_b32 v" #r ", " v "\n\t"
#define XM4(a, b, c, d, v) XM(a, v) XM(b, v) XM(c, v) XM(d, v)
#define CNT_END "8"
        STAMP(3);
        asm volatile(
            "ds_read_b128 v[132:135], %[kp]\n\t"
            XM4(112, 113, 114, 115, "%[nm]") XM4(116, 117, 118, 119, "%[nm]") XM4(120, 121, 122, 123, "%[nm]") XM4(124, 125, 126, 127, "%[nm]")
            XM4(96, 97, 98, 99, "0") XM4(100, 101, 102, 103, "0") XM4(104, 105, 106, 107, "0") XM4(108, 109, 110, 111, "0")
            XM4(88, 89, 90, 91, "0") XM4(92, 93, 94, 95, "0") XM4(140, 141, 142, 143, "0") XM(144, "0xdddd")
            "s_waitcnt lgkmcnt(0)\n\t"
            "v_mfma_f32_32x32x16_bf16 v[64:79], v[132:135], %[q], v[112:127]\n\t"
            "ds_read_b128 v[132:135], %[kp] offset:1024\n\t"
            "ds_read_b128 v[136:139], %[vp]\n\t"
            "s_nop 9\n\t"
            "1:\n\t"
            ASTEP(64, 80, 0) ASTEP(80, 64, 1) ASTEP(64, 80, 2) ASTEP(80, 64, 3) ASTEP(64, 80, 4) ASTEP(80, 64, 5)
            "s_cmp_gt_u32 %[cnt], 4\n\t"
            "s_cbranch_scc1 2f\n\t"
            "s_waitcnt vmcnt(2)\n\t"
            "s_barrier\n\t"
            "s_mov_b32 m0, %[ld]\n\t"
            "s_add_u32 %[ld], %[ld], 0x10000\n\t"
            "global_load_lds_dwordx4 %[l16], %[kb]\n\t"
            "s_mov_b32 m0, %[ld]\n\t"
            "s_sub_u32 %[ld], %[ld], 0xe000\n\t"
            "global_load_lds_dwordx4 %[l16], %[vb]\n\t"
            "v_add_u32 %[l16], 0x2000, %[l16]\n\t"
            "s_branch 3f\n\t"
            "2:\n\t"
            "s_cmp_gt_u32 %[cnt], 5\n\t"
            "s_cbranch_scc1 3f\n\t"
            "s_waitcnt vmcnt(0)\n\t"
            "s_barrier\n\t"
            "3:\n\t"
            ASTEP(64, 80, 6) ASTEP(80, 64, 7)
            "v_add_u32 %[kp], 0x2000, %[kp]\n\t"
            "v_add_u32 %[vp], %[vs], %[vp]\n\t"
            "s_add_u32 %[cnt], %[cnt], 1\n\t"
            "s_cmp_lt_u32 %[cnt], " CNT_END "\n\t"
            "s_cbranch_scc1 1b\n\t"
            "s_waitcnt lgkmcnt(0)\n\t"
            "v_smfmac_f32_32x32x32_bf16 v[96:111], v[140:143], v[88:95], v144\n\t"
            "s_nop 11\n\t"
            "v_mov_b32 %[o0], v96\n\tv_mov_b32 %[o1], v97\n\tv_mov_b32 %[o2], v98\n\tv_mov_b32 %[o3], v99\n\t"
            "v_mov_b32 %[o4], v100\n\tv_mov_b32 %[o5], v101\n\tv_mov_b32 %[o6], v102\n\tv_mov_b32 %[o7], v103\n\tv_mov_b32 %[o8], v104\n\t"
            : [o0] "=&v"(o0), [o1] "=&v"(o1), [o2] "=&v"(o2), [o3] "=&v"(o3), [o4] "=&v"(o4), [o5] "=&v"(o5), [o6] "=&v"(o6), [o7] "=&v"(o7), [o8] "=&v"(o8),
              [kp] "+v"(kpa), [vp] "+v"(vpa), [l16] "+v"(l16), [ld] "+s"(ldst), [cnt] "+s"(cnt)
            : [q] "v"(qf), [nm] "v"(negm), [vs] "v"(vstep), [kb] "s"(kb), [vb] "s"(vb)
            : "memory", "scc", "m0",
              "v64", "v65", "v66", "v67", "v68", "v69", "v70", "v71", "v72", "v73", "v74", "v75", "v76", "v77", "v78", "v79",
              "v80", "v81", "v82", "v83", "v84", "v85", "v86", "v87", "v88", "v89", "v90", "v91", "v92", "v93", "v94", "v95",
              "v96", "v97", "v98", "v99", "v100", "v101", "v102", "v103", "v104", "v105", "v106", "v107", "v108", "v109", "v110", "v111",
              "v112", "v113", "v114", "v115", "v116", "v117", "v118", "v119", "v120", "v121", "v122", "v123", "v124", "v125", "v126", "v127",
              "v132", "v133", "v134", "v135", "v136", "v137", "v138", "v139", "v140", "v141", "v142", "v143", "v144");
        STAMP(4);
        o[0] = o0; o[1] = o1; o[2] = o2; o[3] = o3; o[4] = o4; o[5] = o5; o[6] = o6; o[7] = o7; o[8] = o8;
    }
    u32x4 qA = *(const u32x4*)(ws + WS_Q + hA + qt * 1024 + lane * 16);
    u32x4 kA0 = *(const u32x4*)(ws + WS_K + hA + qp * 1024 + lane * 16);
    u32x4 kA1 = *(const u32x4*)(ws + WS_K + hA + qt * 1024 + lane * 16);
    u32x4 vA0 = *(const u32x4*)(ws + WS_V + hA + qp * 1024 + 512 + vl);
    u32x4 vA1 = *(const u32x4*)(ws + WS_V + hA + qt * 1024 + vl);
    u32x4 vA2 = *(const u32x4*)(ws + WS_V + hA + qt * 1024 + 512 + vl);
    float gate = *(const float*)(ws + WS_G + (qt * 32 + r32) * 4);
    STAMP(7);
    {
        float chk = o[8];
#pragma unroll
        for (int r = 0; r < 8; ++r) chk = fmaxf(chk, fabsf(o[r]));
        const bool bad = !(chk < 1e30f) || !(o[0] + o[1] + o[2] + o[3] + o[4] + o[5] + o[6] + o[7] + o[8] < 1e37f);
        if (__any(bad)) {
            kp = (lds_cptr)lds + lane * 16;
            vp = (lds_cptr)lds + (r32 < 16 ? K2_V : K2_ONES) + vl;
            const int vs1 = r32 < 16 ? 1024 : 0;
            float m = -INFINITY; o = f32x16{};
#pragma unroll 1
            for (int t = 0; t < 64; ++t) {
                f32x16 z = {}; f32x16 s = MF32(lds_ld16(kp), qf, z);
                float tm = s[0];
#pragma unroll
                for (int r = 1; r < 16; ++r) tm = fmaxf(tm, s[r]);
                const float mn = fmaxf(m, halfmax(tm));
                const float al = __builtin_amdgcn_exp2f(m - mn); m = mn;
#pragma unroll
                for (int r = 0; r < 16; ++r) { s[r] = __builtin_amdgcn_exp2f(s[r] - mn); o[r] *= al; }
                o = SMF(lds_ld16(vp), LO8(s), o); o = SMF(lds_ld16(vp + 512), HI8(s), o);
                kp += 1024; vp += vs1;
            }
        }
    }
    char* od = ws + WS_O + (size_t)qt * 65536 + lane * 16;
    store_o(od + (NH + head) * 1024, o, bf16r(1.f - gate) * __builtin_amdgcn_rcpf(o[8]));

    STAMP(8);
    {
        const f32x16 z = {};
        const bf16x8 qa = __builtin_bit_cast(bf16x8, qA);
        f32x16 s0 = MF32(__builtin_bit_cast(bf16x8, kA0), qa, z), s1 = MF32(__builtin_bit_cast(bf16x8, kA1), qa, z);
        float m = -INFINITY;
#pragma unroll
        for (int r = 0; r < 16; ++r) {
            const int d1 = r32 - crow(r, hh);
            s1[r] = (d1 >= 0 && d1 <= 14 && !(d1 & 1)) ? s1[r] : -INFINITY; m = fmaxf(m, s1[r]);
            if (r >= 8) { const int d0 = d1 + 32; s0[r] = (qt > 0 && d0 <= 14 && !(d0 & 1)) ? s0[r] : -INFINITY; m = fmaxf(m, s0[r]); }
        }
        m = halfmax(m);
#pragma unroll
        for (int r = 0; r < 16; ++r) { s1[r] = __builtin_amdgcn_exp2f(s1[r] - m); if (r >= 8) s0[r] = __builtin_amdgcn_exp2f(s0[r] - m); }
        const u32x4 ones = {0x3f803f80u, 0x3f803f80u, 0x3f803f80u, 0x3f803f80u};
        if (r32 >= 16) { vA0 = ones; vA1 = ones; vA2 = ones; }
        f32x16 oa = {};
        oa = SMF(__builtin_bit_cast(bf16x8, vA0), HI8(s0), oa);
        oa = SMF(__builtin_bit_cast(bf16x8, vA1), LO8(s1), oa);
        oa = SMF(__builtin_bit_cast(bf16x8, vA2), HI8(s1), oa);
        store_o(od + head * 1024, oa, gate * __builtin_amdgcn_rcpf(oa[8]));
    }
    STAMP(9); STAMP_FLUSH(ws, 1);
}

__global__ __launch_bounds__(512, 2) void k3_out(const char* __restrict__ ws, __hip_bfloat16* __restrict__ out) {
    extern __shared__ __attribute__((aligned(16))) char lds[];
    const int tid = threadIdx.x, lane = tid & 63, r32 = lane & 31, hh = lane >> 5;
    const int wid = __builtin_amdgcn_readfirstlane(tid >> 6);
    const int b = blockIdx.x;
    STAMP_DECL; STAMP(0);
    const int rb = (b & 7) * 4 + ((b >> 3) & 3), nb = b >> 5;
    const char* osrc = ws + WS_O + (size_t)rb * 2 * 65536 + wid * 8192 + lane * 16;
    const char* wsrc = ws + WS_W + (size_t)wid * 8 * 16384 + nb * 2048 + lane * 16;
    bf16x8 of[2][8], wf[2][8];
#pragma unroll
    for (int k = 0; k < 8; ++k)
#pragma unroll
        for (int i = 0; i < 2; ++i) {
            of[i][k] = *(const bf16x8*)(osrc + i * 65536 + k * 1024);
            wf[i][k] = *(const bf16x8*)(wsrc + k * 16384 + i * 1024);
        }
    f32x16 acc[2][2] = {};
#pragma unroll
    for (int k = 0; k < 8; ++k)
#pragma unroll
        for (int m = 0; m < 2; ++m)
#pragma unroll
            for (int n = 0; n < 2; ++n) acc[m][n] = MF32(wf[n][k], of[m][k], acc[m][n]);
    STAMP(1);
    float* red = (float*)lds;
#pragma unroll
    for (int m = 0; m < 2; ++m)
#pragma unroll
        for (int n = 0; n < 2; ++n)
#pragma unroll
            for (int q = 0; q < 4; ++q)
                *(f32x4*)(red + wid * 4096 + ((m * 2 + n) * 4 + q) * 256 + lane * 4) =
                    f32x4{acc[m][n][4 * q], acc[m][n][4 * q + 1], acc[m][n][4 * q + 2], acc[m][n][4 * q + 3]};
    __syncthreads();
    STAMP(2);
    f32x4 s0 = {}, s1 = {};
#pragma unroll
    for (int p = 0; p < 8; ++p) {
        s0 += *(const f32x4*)(red + p * 4096 + (2 * wid) * 256 + lane * 4);
        s1 += *(const f32x4*)(red + p * 4096 + (2 * wid + 1) * 256 + lane * 4);
    }
    unsigned a0 = cvtpk(s0[0], s0[1]), a1 = cvtpk(s0[2], s0[3]), b0 = cvtpk(s1[0], s1[1]), b1 = cvtpk(s1[2], s1[3]);
    auto r0 = __builtin_amdgcn_permlane32_swap(a0, b0, false, false);
    auto r1 = __builtin_amdgcn_permlane32_swap(a1, b1, false, false);
    const int tile = wid >> 1, m = tile >> 1, n = tile & 1;
    const int row = rb * 64 + m * 32 + r32, col = nb * 64 + n * 32 + (wid & 1) * 16 + hh * 8;
    *(u32x4*)((char*)out + (size_t)row * 1024 + col * 2) = u32x4{r0[0], r1[0], r0[1], r1[1]};
    STAMP(3); STAMP_FLUSH(ws, 2);
}

extern "C" void kernel_launch(void* const* d_in, const int* in_sizes, int n_in, void* d_out, int out_size, void* d_ws,
                              size_t ws_size, hipStream_t stream) {
    const __hip_bfloat16* x = (const __hip_bfloat16*)d_in[0];
    const __hip_bfloat16* wA = (const __hip_bfloat16*)d_in[1];
    const __hip_bfloat16* woA = (const __hip_bfloat16*)d_in[2];
    const __hip_bfloat16* wB = (const __hip_bfloat16*)d_in[4];
    const __hip_bfloat16* woB = (const __hip_bfloat16*)d_in[5];
    const __hip_bfloat16* gw = (const __hip_bfloat16*)d_in[6];
    (void)hipFuncSetAttribute((const void*)k1_qkv, hipFuncAttributeMaxDynamicSharedMemorySize, K1_STAGE * K1_NST);
    (void)hipFuncSetAttribute((const void*)k2_attn, hipFuncAttributeMaxDynamicSharedMemorySize, K2_LDS);
    (void)hipFuncSetAttribute((const void*)k3_out, hipFuncAttributeMaxDynamicSharedMemorySize, 131072);
    k1_qkv<<<256, 512, K1_STAGE * K1_NST, stream>>>(x, wA, wB, woA, woB, gw, (char*)d_ws);
    k2_attn<<<256, 512, K2_LDS, stream>>>((char*)d_ws);
    k3_out<<<256, 512, 131072, stream>>>((const char*)d_ws, (__hip_bfloat16*)d_out);
}
```

```cpp
#include <hip/hip_runtime.h>
#include <hip/hip_bf16.h>
#include <stdint.h>
#include <type_traits>

typedef __attribute__((ext_vector_type(8))) __bf16 bf16x8;
typedef __attribute__((ext_vector_type(16))) __bf16 bf16x16;
typedef __attribute__((ext_vector_type(4))) short s16x4;
typedef __attribute__((ext_vector_type(4))) float f32x4;
typedef __attribute__((ext_vector_type(8))) float f32x8;
typedef __attribute__((ext_vector_type(16))) float f32x16;
typedef __attribute__((ext_vector_type(2))) unsigned u32x2;
typedef __attribute__((ext_vector_type(4))) unsigned u32x4;
typedef __attribute__((address_space(3))) const char* lds_cptr;
typedef __attribute__((address_space(3))) char* lds_ptr;

constexpr int SEQ = 2048, DM = 512, NH = 32;
constexpr float QSCALE = 0.25f * 1.4426950408889634f;

constexpr size_t WS_Q = 0;
constexpr size_t WS_K = WS_Q + (2u << 20);
constexpr size_t WS_V = WS_K + (2u << 20);
constexpr size_t WS_O = WS_V + (2u << 20);
constexpr size_t WS_W = WS_O + (4u << 20);
constexpr size_t WS_G = WS_W + (1u << 20);
constexpr int FS = 128;
constexpr size_t WS_FB = WS_G + 8192;
constexpr size_t WS_FA = WS_FB + 256 * FS;
constexpr size_t WS_F2 = WS_FA + 256 * FS;
constexpr size_t WS_E = WS_F2 + 256 * FS;

#define STAMP_DECL do {} while (0)
#define STAMP(i) do {} while (0)
#define STAMP_FLUSH(ws, kid) do {} while (0)
#define PIN(x) asm volatile("" : "+v"(x))
#define PIN8(d) asm volatile("" : "+v"(d[0]), "+v"(d[1]), "+v"(d[2]), "+v"(d[3]), "+v"(d[4]), "+v"(d[5]), "+v"(d[6]), "+v"(d[7]))
#define SBAR() __builtin_amdgcn_sched_barrier(0)

__device__ __forceinline__ unsigned cvtpk(float lo, float hi) {
    typedef __attribute__((ext_vector_type(2))) float f32x2_; typedef __attribute__((ext_vector_type(2))) __bf16 bf16x2_;
    return __builtin_bit_cast(unsigned, __builtin_convertvector(f32x2_{lo, hi}, bf16x2_));
}
__device__ __forceinline__ float bf16r(float x) { return __uint_as_float(cvtpk(x, x) << 16); }
__device__ __forceinline__ float bflo(unsigned u) { return __uint_as_float(u << 16); }
__device__ __forceinline__ float bfhi(unsigned u) { return __uint_as_float(u & 0xffff0000u); }
__device__ __forceinline__ void glds16s(const void* sbase, unsigned voff, unsigned lds_base) {
    asm volatile("s_mov_b32 m0, %2\n\ts_nop 0\n\tglobal_load_lds_dwordx4 %0, %1" :: "v"(voff), "s"(sbase), "s"(lds_base) : "memory");
}
__device__ __forceinline__ void glds16d(const void* sbase, unsigned voff, unsigned lds_base) {
    asm volatile("s_mov_b32 m0, %2\n\ts_nop 0\n\tglobal_load_lds_dwordx4 %0, %1 sc1" :: "v"(voff), "s"(sbase), "s"(lds_base) : "memory");
}
__device__ __forceinline__ void st16(void* p, u32x4 v) {
    asm volatile("global_store_dwordx4 %0, %1, off sc1\n\ts_nop 1" :: "v"(p), "v"(v) : "memory");
}
typedef __attribute__((address_space(1))) unsigned long long gu64;
typedef __attribute__((address_space(1))) unsigned gu32;
#define RLX_AGENT __ATOMIC_RELAXED, __HIP_MEMORY_SCOPE_AGENT
#define LAS __attribute__((address_space(3)))
#define ACQ 0
__device__ __forceinline__ void acquire_agent() {
    __builtin_amdgcn_fence(__ATOMIC_ACQUIRE, "wavefront");
}
constexpr unsigned SPIN_CAP = 1u << 17;
__device__ __forceinline__ bf16x8 lds_ld16(lds_cptr p) { return *(const __attribute__((address_space(3))) bf16x8*)p; }
__device__ __forceinline__ s16x4 lds_tr(lds_cptr p) {
    return __builtin_amdgcn_ds_read_tr16_b64_v4i16((__attribute__((address_space(3))) s16x4*)p);
}

#define CODE_PF 22016
#define LPRIO 3
#define WA_PF 1
#define PSLEEP 1
#define HSLEEP 2
constexpr int XSLAB = 10240, L_W = 8 * XSLAB, WSLAB = 24576, L_CTL = L_W + 3 * WSLAB + 2048, L_WO = L_CTL + 128, L_PL = L_WO + 4096, LDS_BYTES = L_PL + 1024;
constexpr int L_Q = 0, L_ONES = 8192, L_KV = 16384, L_PA = L_KV + 8 * 16384, PA_LD = 144, L_GT = L_PA + 64 * PA_LD;
static_assert(L_GT + 256 <= L_CTL && L_KV + 2 * 16384 <= 5 * XSLAB, "LDS map");
#define MF16(a, b, c) __builtin_amdgcn_mfma_f32_16x16x32_bf16(a, b, c, 0, 0, 0)
#define MF16K16(a, b, c) __builtin_amdgcn_mfma_f32_16x16x16bf16_1k(__builtin_bit_cast(s16x4, a), __builtin_bit_cast(s16x4, b), c, 0, 0, 0)
#define BAR_L() asm volatile("s_waitcnt lgkmcnt(0)\n\ts_barrier" ::: "memory")

__device__ __forceinline__ u32x2 pack4(const f32x4& a) { return u32x2{cvtpk(a[0], a[1]), cvtpk(a[2], a[3])}; }
__device__ __forceinline__ u32x4 join2(u32x2 a, u32x2 b) {
    auto r0 = __builtin_amdgcn_permlane16_swap(a[0], b[0], false, false);
    auto r1 = __builtin_amdgcn_permlane16_swap(a[1], b[1], false, false);
    return u32x4{r0[0], r1[0], r0[1], r1[1]};
}
#define MX(a, b) __builtin_elementwise_maximum(a, b)
#define FU(x) __float_as_uint(x)
#define UF(x) __uint_as_float(x)
__device__ __forceinline__ void xmax4x4(float (&m)[4]) {
    auto a = __builtin_amdgcn_permlane32_swap(FU(m[0]), FU(m[1]), false, false);
    auto b = __builtin_amdgcn_permlane32_swap(FU(m[2]), FU(m[3]), false, false);
    auto c = __builtin_amdgcn_permlane16_swap(FU(MX(UF(a[0]), UF(a[1]))), FU(MX(UF(b[0]), UF(b[1]))), false, false);
    const unsigned y = FU(MX(UF(c[0]), UF(c[1])));
    auto d = __builtin_amdgcn_permlane16_swap(y, y, false, false);
    auto e = __builtin_amdgcn_permlane32_swap(d[0], d[0], false, false);
    auto f = __builtin_amdgcn_permlane32_swap(d[1], d[1], false, false);
    m[0] = UF(e[0]); m[1] = UF(e[1]); m[2] = UF(f[0]); m[3] = UF(f[1]);
}

#define BF8(v) __builtin_bit_cast(bf16x8, v)
template <bool HALO, bool TAILBAR>
__device__ __forceinline__ void proj_pass(lds_cptr lds, int xo, int wo, int wsl, f32x4 (&aq)[4], f32x4 (&ak)[5], f32x4 (&av)[5]) {
    constexpr int M0 = HALO ? 0 : 1, NRD = 6 + 5 - M0, NMF = 12 + (HALO ? 2 : 0);
    u32x4 xa[5], wa[3], xb[5], wb[3];
    auto ld = [&](u32x4 (&xf)[5], u32x4 (&wf)[3], int xs, int kk) {
        xf[1] = *(const LAS u32x4*)(lds + xs + (xo ^ (kk * 64)) + 2048);
#pragma unroll
        for (int s = 0; s < 3; ++s) {
            const u32x2 lo = __builtin_bit_cast(u32x2, lds_tr(lds + wsl + wo + s * 8192 + kk * 4096));
            const u32x2 hi = __builtin_bit_cast(u32x2, lds_tr(lds + wsl + wo + s * 8192 + kk * 4096 + 512));
            wf[s] = u32x4{lo[0], lo[1], hi[0], hi[1]};
        }
#pragma unroll
        for (int m = 2; m < 5; ++m) xf[m] = *(const LAS u32x4*)(lds + xs + (xo ^ (kk * 64)) + m * 2048);
        if (HALO) xf[0] = *(const LAS u32x4*)(lds + xs + (xo ^ (kk * 64)));
    };
    auto mm1 = [&](u32x4 (&xf)[5], u32x4 (&wf)[3], int m) {
        if (m) aq[m - 1] = MF16(BF8(wf[0]), BF8(xf[m]), aq[m - 1]);
        ak[m] = MF16(BF8(wf[1]), BF8(xf[m]), ak[m]);
        av[m] = MF16(BF8(xf[m]), BF8(wf[2]), av[m]);
    };
    auto rest = [&](u32x4 (&xf)[5], u32x4 (&wf)[3]) { mm1(xf, wf, 2); mm1(xf, wf, 3); mm1(xf, wf, 4); if (HALO) mm1(xf, wf, 0); };
    auto weave = [&]() {
        __builtin_amdgcn_sched_group_barrier(0x100, NRD - (NMF - 3) + 1, 0);
#pragma unroll
        for (int i = 0; i < NMF - 4; ++i) { __builtin_amdgcn_sched_group_barrier(0x8, 1, 0); __builtin_amdgcn_sched_group_barrier(0x100, 1, 0); }
        __builtin_amdgcn_sched_group_barrier(0x8, 1, 0);
    };
#define WBAR() do { __builtin_amdgcn_s_waitcnt(0xC07F); __builtin_amdgcn_s_barrier(); asm volatile("" ::: "memory"); SBAR(); } while (0)
    WBAR();
    ld(xa, wa, 0, 0); SBAR();
#pragma unroll 1
    for (int t = 0; t < 7; ++t) {
        mm1(xa, wa, 1); SBAR();
        __builtin_amdgcn_s_waitcnt(0xC07F); SBAR();
        ld(xb, wb, t * XSLAB, 1); rest(xa, wa); weave(); SBAR();
        wsl = wsl == 2 * WSLAB ? 0 : wsl + WSLAB;
        mm1(xb, wb, 1); SBAR(); WBAR();
        ld(xa, wa, (t + 1) * XSLAB, 0); rest(xb, wb); weave(); SBAR();
    }
    mm1(xa, wa, 1); SBAR();
    __builtin_amdgcn_s_waitcnt(0xC07F); SBAR();
    ld(xb, wb, 7 * XSLAB, 1); rest(xa, wa); weave(); SBAR();
    mm1(xb, wb, 1); SBAR();
    if (TAILBAR) WBAR();
    rest(xb, wb);
}

#define SMF(a, plo, acc) __builtin_amdgcn_smfmac_f32_32x32x32_bf16(a, __builtin_bit_cast(bf16x16, plo), acc, 0xDDDD, 0, 0)
#define LO8(p) __builtin_shufflevector(p, p, 0, 1, 2, 3, 4, 5, 6, 7)
#define HI8(p) __builtin_shufflevector(p, p, 8, 9, 10, 11, 12, 13, 14, 15)
#define MF32(a, b, c) __builtin_amdgcn_mfma_f32_32x32x16_bf16(a, b, c, 0, 0, 0)
#define BAR() asm volatile("s_barrier" ::: "memory")

__device__ __forceinline__ float halfmax(float m) {
    auto rr = __builtin_amdgcn_permlane32_swap(FU(m), FU(m), false, false);
    return MX(UF(rr[0]), UF(rr[1]));
}
__device__ __forceinline__ void publish(char* lds, int cnt_off, unsigned last, char* flag, int lane) {
    asm volatile("s_waitcnt vmcnt(0)" ::: "memory");
    if (lane == 0) {
        const unsigned old = __hip_atomic_fetch_add((LAS unsigned*)((lds_ptr)lds + cnt_off), 1u, __ATOMIC_RELAXED, __HIP_MEMORY_SCOPE_WORKGROUP);
        if (old == last) __hip_atomic_store((gu64*)flag, *(volatile LAS unsigned long long*)((lds_ptr)lds + L_CTL), RLX_AGENT);
    }
}

__global__ __launch_bounds__(768, 3) void fused(const __hip_bfloat16* __restrict__ x, const __hip_bfloat16* __restrict__ wA,
                                                const __hip_bfloat16* __restrict__ wB, const __hip_bfloat16* __restrict__ woA,
                                                const __hip_bfloat16* __restrict__ woB, const __hip_bfloat16* __restrict__ gw,
                                                char* ws, __hip_bfloat16* __restrict__ out) {
    extern __shared__ __attribute__((aligned(16))) char lds[];
    unsigned long long pcv; asm volatile("s_getpc_b64 %0" : "=s"(pcv));
    const int tid = threadIdx.x, lane = tid & 63;
    const int wid = __builtin_amdgcn_readfirstlane(tid >> 6);
    const int b = blockIdx.x;
    STAMP_DECL; STAMP(0);
    const int hg = (b & 6) | ((b >> 3) & 1), rt = (b & 1) * 16 + (b >> 4);
    const int head = (b & 7) * 4 + ((b >> 3) & 3), qb = b >> 5;
    const int r64 = head, nb = qb;
    const unsigned lds0 = (unsigned)(uintptr_t)lds, l16 = lane * 16;
    const size_t hB = (size_t)head * 65536;

    const int pr = lane >> 3, pc = lane & 7;
    const int rot = hg + rt;
    if (wid >= 4) {
        const int j = wid - 4;
        const unsigned vw = pr * 3072 + (((pc >> 1) ^ (((pr >> 1) & 1) | ((j & 1) << 1))) << 5) + ((pc & 1) << 4);
        const unsigned vx = pr * 1024 + ((pc ^ ((4 * (j & 1) + (pr >> 1)) & 7)) << 4);
        const char* xg = (const char*)x + (size_t)(rt * 64 + j * 8) * 1024;
        const char* wg = (const char*)wB + hg * 128 + j * 8 * 3072;
        int so = 0;
#pragma unroll 1
        for (int t = 0; t < 8; ++t) {
            if (t >= 2) asm volatile("s_waitcnt vmcnt(4)\n\ts_barrier" ::: "memory");
            const int kt = (t + rot) & 7;
#pragma unroll
            for (int i = 0; i < 3; ++i) glds16s(wg + (size_t)kt * 64 * 3072 + i * 1024, vw, lds0 + L_W + so + i * 8192 + j * 1024);
            glds16s(xg + kt * 128, vx, lds0 + t * XSLAB + 2048 + j * 1024);
            so = so == 2 * WSLAB ? 0 : so + WSLAB;
        }
    }
    if (wid >= 8) {
        const int hw = wid - 8;
        __builtin_amdgcn_s_setprio(3);
        const char* fkv = ws + WS_FB + (head >> 2) * 32 * FS;
        {
            unsigned vw[2];
#pragma unroll
            for (int p = 0; p < 2; ++p) vw[p] = pr * 3072 + (((pc >> 1) ^ (((pr >> 1) & 1) | (p << 1))) << 5) + ((pc & 1) << 4);
            const unsigned vh = pr * 1024 + ((pc ^ ((4 * (hw & 1) + (pr >> 1)) & 7)) << 4);
            const char* xh = (const char*)x + (size_t)((rt ? rt * 64 - 16 : 0) + (hw & 1) * 8) * 1024;
            const unsigned lw = lds0 + L_W + hw * 2048;
            int so = 2 * WSLAB;
#pragma unroll 1
            for (int t = 0; t < 8; ++t) {
                if (!t) asm volatile("s_waitcnt vmcnt(4)" ::: "memory");
                BAR();
                const int kt = (t + rot) & 7;
                const char* wg = (const char*)wA + hg * 128 + (size_t)(kt * 64 + hw * 16) * 3072;
                if (hw < 2) glds16s(xh + kt * 128, vh, lds0 + t * XSLAB + hw * 1024);
#pragma unroll
                for (int i = 0; i < 6; ++i) glds16s(wg + (i >> 1) * 1024 + (i & 1) * 8 * 3072, vw[i & 1], lw + so + (i >> 1) * 8192 + (i & 1) * 1024);
                so = so == 2 * WSLAB ? 0 : so + WSLAB;
                if (hw < 2) asm volatile("s_waitcnt vmcnt(7)" ::: "memory"); else asm volatile("s_waitcnt vmcnt(6)" ::: "memory");
            }
            asm volatile("s_barrier\n\ts_waitcnt vmcnt(0)\n\ts_barrier" ::: "memory");
        }
        STAMP(2);
        __builtin_amdgcn_s_setprio(3);
        const unsigned long long tag = *(volatile LAS unsigned long long*)((lds_ptr)lds + L_CTL);
        unsigned spins = 0, ready = 0;
        auto quads = [](unsigned long long bal) { unsigned v = (unsigned)bal; v &= v >> 1; v &= v >> 2; unsigned r = 0;
#pragma unroll
            for (int c = 0; c < 8; ++c) r |= ((v >> (4 * c)) & 1u) << c;
            return r; };
        volatile LAS unsigned* seen = (volatile LAS unsigned*)((lds_ptr)lds + L_CTL + 16);
        auto await = [&](unsigned m, const char* fbase, int nl, bool kv) {
            if ((ready & m) == m) return;
            if (hw == 3) {
                for (;;) {
                    bool ok = __hip_atomic_load((gu64*)(fbase + (lane & (nl - 1)) * FS), RLX_AGENT) == tag;
                    if (nl == 256) {
#pragma unroll
                        for (int i = 1; i < 4; ++i) ok &= __hip_atomic_load((gu64*)(fbase + (i * 64 + lane) * FS), RLX_AGENT) == tag;
                    }
                    const unsigned long long bal = __ballot(ok);
                    ready |= kv ? quads(bal) : (bal == ~0ull ? m : 0u);
                    if ((ready & m) == m) break;
                    if (++spins > SPIN_CAP) { ready = ~0u; break; }
                    __builtin_amdgcn_s_sleep(PSLEEP);
                }
                *seen = ready;
            } else {
                for (;;) { ready |= __builtin_amdgcn_readfirstlane(*seen); if ((ready & m) == m) break; __builtin_amdgcn_s_sleep(1); }
            }
            acquire_agent();
        };
        const char* ksrc = ws + WS_K + hB + hw * 2048;
        const char* vsrc = ws + WS_V + hB + hw * 2048;
        const unsigned ldsw = lds0 + L_KV + hw * 2048;
        auto chunk = [&](int s) {
            const int c = (qb + s) & 7;
            await(1u << c, fkv, 32, true);
            glds16d(ksrc + c * 8192, l16, ldsw + s * 16384); glds16d(vsrc + c * 8192, l16, ldsw + 8192 + s * 16384);
            glds16d(ksrc + c * 8192 + 1024, l16, ldsw + s * 16384 + 1024); glds16d(vsrc + c * 8192 + 1024, l16, ldsw + 8192 + s * 16384 + 1024);
        };
        STAMP(3);
        {
            const unsigned one = (lane & 11) == 0 ? 0x3f803f80u : 0u; const u32x4 cv = {one, one, one, one};
            *(LAS u32x4*)((lds_ptr)lds + L_ONES + hw * 2048 + l16) = cv; *(LAS u32x4*)((lds_ptr)lds + L_ONES + hw * 2048 + 1024 + l16) = cv; }
        BAR_L();
        STAMP(4);
        chunk(1);
        const int r32 = lane & 31, hh = lane >> 5, t32 = r64 * 2 + (hw >> 1), n32 = nb * 2 + (hw & 1);
        const char* osrc = ws + WS_O + (size_t)t32 * 65536 + 4096;
        const char* wsrc = ws + WS_W + n32 * 65536 + 4096;
        u32x4 of[8], wf[8]; float gate;
#define LD8(d, base) asm volatile( \
            "global_load_dwordx4 %0, %8, %9 offset:-4096 sc1\n\tglobal_load_dwordx4 %1, %8, %9 offset:-3072 sc1\n\t" \
            "global_load_dwordx4 %2, %8, %9 offset:-2048 sc1\n\tglobal_load_dwordx4 %3, %8, %9 offset:-1024 sc1\n\t" \
            "global_load_dwordx4 %4, %8, %9 sc1\n\tglobal_load_dwordx4 %5, %8, %9 offset:1024 sc1\n\t" \
            "global_load_dwordx4 %6, %8, %9 offset:2048 sc1\n\tglobal_load_dwordx4 %7, %8, %9 offset:3072 sc1" \
            : "=&v"(d[0]), "=&v"(d[1]), "=&v"(d[2]), "=&v"(d[3]), "=&v"(d[4]), "=&v"(d[5]), "=&v"(d[6]), "=&v"(d[7]) : "v"(l16), "s"(base) : "memory")
        f32x16 acc = {};
        asm volatile("s_waitcnt vmcnt(0)\n\ts_barrier" ::: "memory");
        if (tid == 704) __hip_atomic_store((gu64*)(ws + WS_FA + b * FS), tag, RLX_AGENT);
        chunk(2);
        asm volatile("s_waitcnt vmcnt(0)\n\ts_barrier" ::: "memory");
        chunk(3);
        await(0x100u, ws + WS_FA, 256, false);
        ready |= 0xffu;
        if (b == 0 && tid == 512) __hip_atomic_store((gu64*)(ws + WS_E), tag, RLX_AGENT);
        asm volatile("global_load_dword %0, %1, %2 sc1" : "=v"(gate) : "v"(r32 * 4), "s"(ws + WS_G + t32 * 128) : "memory");
        LD8(of, osrc); LD8(wf, wsrc);
#pragma unroll
        for (int c = 2; c <= 5; ++c) {
            asm volatile("s_waitcnt vmcnt(16)\n\ts_barrier" ::: "memory");
            chunk(c + 2);
            asm volatile("s_waitcnt vmcnt(4)" ::: "memory");
            PIN8(of); PIN8(wf);
#pragma unroll
            for (int k = 0; k < 8; ++k) {
                acc = MF32(BF8(wf[k]), BF8(of[k]), acc); PIN(acc); SBAR();
                __builtin_amdgcn_s_sleep(HSLEEP); SBAR();
            }
            if (c < 5) { LD8(of, osrc + (c - 1) * 8192); LD8(wf, wsrc + (c - 1) * 8192); }
        }
        asm volatile("s_waitcnt vmcnt(0)\n\ts_barrier" ::: "memory");
        { const char* src = ws + WS_W + (nb * 2 + (hw >> 1)) * 65536 + (32 + (hw & 1) * 16) * 1024;
          const unsigned dst = lds0 + L_KV + hw * 16384;
#pragma unroll 1
          for (int i = 0; i < 16; ++i) glds16d(src + i * 1024, l16, dst + i * 1024); }
        { PIN(acc); PIN(gate);
          lds_ptr dst = (lds_ptr)lds + L_PA + ((hw >> 1) * 32 + r32) * PA_LD + (hw & 1) * 64 + hh * 8;
#pragma unroll
          for (int q = 0; q < 4; ++q)
              *(LAS u32x2*)(dst + q * 16) = u32x2{cvtpk(gate * bf16r(acc[4 * q]), gate * bf16r(acc[4 * q + 1])),
                                                  cvtpk(gate * bf16r(acc[4 * q + 2]), gate * bf16r(acc[4 * q + 3]))};
          *(LAS float*)((lds_ptr)lds + L_GT + ((hw >> 1) * 32 + r32) * 4) = gate; }
        BAR();
        STAMP(5);
        BAR_L();
        STAMP(6);
        await(0x1000u, ws + WS_F2 + (r64 >> 2) * 32 * FS, 32, false);
        STAMP(7);
        { const char* src = ws + WS_O + (size_t)(r64 * 2) * 65536 + (32 + hw * 8) * 1024;
          const unsigned dst = lds0 + L_KV + 65536 + hw * 8192;
#pragma unroll 1
          for (int i = 0; i < 8; ++i) { glds16d(src + i * 1024, l16, dst + i * 1024); glds16d(src + 65536 + i * 1024, l16, dst + 32768 + i * 1024); } }
        asm volatile("s_waitcnt vmcnt(12)\n\ts_barrier\n\ts_waitcnt vmcnt(8)\n\ts_barrier\n\ts_waitcnt vmcnt(4)\n\ts_barrier\n\ts_waitcnt vmcnt(0)\n\ts_barrier" ::: "memory");
        STAMP(11); STAMP_FLUSH(ws, 0);
        return;
    }

    const int fr = lane & 15, fg = lane >> 4;
    {
        const int hl = wid & 3, h1 = hg * 4 + hl;
        const int xo = fr * 128 + ((fg ^ ((fr >> 1) & 7)) << 4);
        int wo;
        { const int k = 8 * fg + (fr >> 2); const int f = ((k >> 1) & 1) | (((k >> 3) & 1) << 1);
          wo = L_W + k * 128 + ((hl ^ f) << 5) + ((fr & 3) << 3); }
        f32x4 aq[4] = {}, ak[5] = {}, av[5] = {};
        if (wid < 4) {
            const int ks = b >> 2, n32 = (b & 3) * 4 + wid;
            glds16s((const char*)(ks < 32 ? woA : woB) + ((ks & 31) * 16 + wid * 4) * 1024 + (b & 3) * 256, fg * 1024 + fr * 16, lds0 + L_WO + wid * 1024);
            u32x4 gv; unsigned long long ep;
            asm volatile("global_load_dwordx4 %0, %1, %2" : "=v"(gv) : "v"(((pr + rot) & 7) * 128 + pc * 16), "s"(gw) : "memory");
            if (wid >= 2 && tid < 224) {
                const int li = tid - 128; unsigned t_;
                asm volatile("global_load_dword %0, %1, %2\n\ts_waitcnt vmcnt(0)" : "=v"(t_)
                             : "v"(((b >> 4) * 32 + (li & 31)) * 3072 + (li >> 5) * 1024), "s"((const char*)wA + hg * 128) : "memory");
            }
            if (tid < 8) {
                const char* ea = tid == 7 ? ws + WS_E : (const char*)(pcv & ~255ull) + min(((b >> 3) + tid * 32) * 128, CODE_PF);
                asm volatile("global_load_dwordx2 %0, %1, off sc1\n\ts_waitcnt vmcnt(0)" : "=v"(ep) : "v"(ea) : "memory");
                if (tid == 7) *(LAS unsigned long long*)((lds_ptr)lds + L_CTL) = ep + 0x9E3779B97F4A7C15ull;
            }
            if (tid >= 64 && tid < 68) *(LAS unsigned*)((lds_ptr)lds + L_CTL + 8 + (tid - 64) * 4) = 0u;
            asm volatile("s_waitcnt vmcnt(0)" : "+v"(gv) :: "memory");
            proj_pass<false, true>((lds_cptr)lds, xo, wo, 0, aq, ak, av);
            STAMP(1);
            const int gr = 16 + hg * 8 + wid * 2;
            const u32x4 gx0 = *(const LAS u32x4*)((lds_cptr)lds + pr * XSLAB + gr * 128 + ((pc ^ ((gr >> 1) & 7)) << 4));
            const u32x4 gx1 = *(const LAS u32x4*)((lds_cptr)lds + pr * XSLAB + gr * 128 + 128 + ((pc ^ ((gr >> 1) & 7)) << 4));
            const size_t off = (size_t)h1 * 65536 + rt * 2048 + l16;
            st16(ws + WS_K + off, join2(pack4(ak[1]), pack4(ak[2]))); st16(ws + WS_K + off + 1024, join2(pack4(ak[3]), pack4(ak[4])));
            st16(ws + WS_V + off, join2(pack4(av[1]), pack4(av[2]))); st16(ws + WS_V + off + 1024, join2(pack4(av[3]), pack4(av[4])));
            st16(ws + WS_Q + off, join2(pack4(aq[0] * QSCALE), pack4(aq[1] * QSCALE)));
            st16(ws + WS_Q + off + 1024, join2(pack4(aq[2] * QSCALE), pack4(aq[3] * QSCALE)));
            BAR_L();
            PIN(gv);
            typedef __attribute__((ext_vector_type(2))) __bf16 bf16x2;
            float z = 0.f, z1 = 0.f;
#pragma unroll
            for (int i = 0; i < 4; ++i) {
                const unsigned x0 = gx0[i], x1 = gx1[i], ga = gv[i];
                z = __builtin_amdgcn_fdot2_f32_bf16(__builtin_bit_cast(bf16x2, x0), __builtin_bit_cast(bf16x2, ga), z, false);
                z1 = __builtin_amdgcn_fdot2_f32_bf16(__builtin_bit_cast(bf16x2, x1), __builtin_bit_cast(bf16x2, ga), z1, false);
            }
            { auto t = __builtin_amdgcn_permlane32_swap(FU(z), FU(z1), false, false); z = UF(t[0]) + UF(t[1]); }
            { auto t = __builtin_amdgcn_permlane16_swap(FU(z), FU(z), false, false); z = UF(t[0]) + UF(t[1]); }
#define DPPADD(v, ctl) v += UF(__builtin_amdgcn_mov_dpp(FU(v), ctl, 0xf, 0xf, true))
            DPPADD(z, 0x140); DPPADD(z, 0x141); DPPADD(z, 0x4e); DPPADD(z, 0xb1);
            float g = bf16r(__builtin_amdgcn_rcpf(1.f + __expf(-bf16r(z))));
            PIN(g);
            const lds_cptr wt = (lds_cptr)lds + L_WO + ((fg >> 1) * 8 + (fr >> 2)) * 256 + wid * 64 + (fg & 1) * 32 + (fr & 3) * 8;
            const u32x2 wlo = __builtin_bit_cast(u32x2, lds_tr(wt)), whi = __builtin_bit_cast(u32x2, lds_tr(wt + 1024));
            const unsigned tagw = *(volatile LAS unsigned*)((lds_ptr)lds + L_CTL + (lane & 1) * 4);
            const char* fsrc = ws + WS_FB + ((head >> 2) * 32 + qb * 4) * FS;
            int st = 0; unsigned spins = 0;
#pragma unroll 1
            for (int u = 10;; ++u) {
                if (st < 2 && (u >= 16 || (__builtin_amdgcn_s_getreg(63495) & 0xC0000Fu) == 0u)) {
                    bool poll = true;
                    if (st == 0) { publish(lds, L_CTL + 8, 3, ws + WS_FB + (hg * 32 + rt) * FS, lane); st = 1; STAMP(2); }
                    else {
                        asm volatile("s_waitcnt vmcnt(0)" ::: "memory");
                        if (__all(*(volatile LAS unsigned*)((lds_ptr)lds + L_PL + tid * 4) == tagw) || ++spins > SPIN_CAP) {
                            poll = false;
                            if (u > 12) {
                                const size_t o2 = hB + qb * 8192 + wid * 2048;
#pragma unroll
                                for (int i = 0; i < 2; ++i) {
                                    glds16d(ws + WS_K + o2 + i * 1024, l16, lds0 + L_KV + wid * 2048 + i * 1024);
                                    glds16d(ws + WS_Q + o2 + i * 1024, l16, lds0 + L_Q + wid * 2048 + i * 1024);
                                    glds16d(ws + WS_V + o2 + i * 1024, l16, lds0 + L_KV + 8192 + wid * 2048 + i * 1024);
                                }
                                st = 2; STAMP(9);
                            }
                        } else if (u >= 16) __builtin_amdgcn_s_sleep(PSLEEP);
                    }
                    if (poll) asm volatile("s_mov_b32 m0, %2\n\ts_nop 0\n\tglobal_load_lds_dword %0, %1 sc1"
                                           :: "v"(((lane >> 1) & 3) * FS + (lane & 1) * 4), "s"(fsrc), "s"(lds0 + L_PL + wid * 256) : "memory");
                }
                if (u < 16) BAR(); else if (st == 2) break;
            }
            st16(ws + WS_W + ((size_t)n32 * 64 + ks) * 1024 + l16, u32x4{wlo[0], wlo[1], whi[0], whi[1]});
            if ((lane & 31) == 0) __hip_atomic_store((gu32*)(ws + WS_G + (rt * 64 + hg * 8 + wid * 2 + (lane >> 5)) * 4), FU(g), RLX_AGENT);
            asm volatile("s_waitcnt vmcnt(2)\n\ts_barrier" ::: "memory");
        } else {
            f32x4 mc, mp;
#pragma unroll
            for (int j = 0; j < 4; ++j) {
                const int d = fr - 4 * fg - j;
                mc[j] = (d >= 0 && !(d & 1)) ? 0.f : -INFINITY;
                mp[j] = (d <= -2 && !(d & 1)) ? 0.f : -INFINITY;
            }
            PIN(mc); PIN(mp);
            asm volatile("s_waitcnt vmcnt(4)\n\ts_barrier\n\ts_waitcnt vmcnt(0)\n\ts_barrier" ::: "memory");
            __builtin_amdgcn_s_setprio(2);
            proj_pass<true, false>((lds_cptr)lds, xo, wo, 2 * WSLAB, aq, ak, av);
            __builtin_amdgcn_s_setprio(0);
            STAMP(2);
            u32x2 qp[4], kp[5], vp[5];
#pragma unroll
            for (int m = 0; m < 4; ++m) qp[m] = pack4(aq[m] * QSCALE);
#pragma unroll
            for (int m = 0; m < 5; ++m) { kp[m] = pack4(ak[m]); vp[m] = pack4(av[m]); }
            const bf16x8 ones = __builtin_bit_cast(bf16x8, u32x4{0x3f803f80u, 0x3f803f80u, 0x3f803f80u, 0x3f803f80u});
            const f32x4 zero = {};
            f32x4 sp[4], sc[4]; float mx[4]; u32x2 op[4];
#pragma unroll
            for (int m = 3; m >= 0; --m) {
                f32x4 mpm = mp;
                if (m == 0 && rt == 0) mpm = f32x4{-INFINITY, -INFINITY, -INFINITY, -INFINITY};
                sc[m] = MF16K16(kp[m + 1], qp[m], mc); sp[m] = MF16K16(kp[m], qp[m], mpm);
            }
#pragma unroll
            for (int m = 3; m >= 0; --m)
                mx[m] = MX(MX(MX(MX(MX(MX(MX(sc[m][0], sc[m][1]), sc[m][2]), sc[m][3]), sp[m][0]), sp[m][1]), sp[m][2]), sp[m][3]);
            xmax4x4(mx);
            PIN(mx[0]); PIN(mx[1]); PIN(mx[2]); PIN(mx[3]);
            BAR();
            PIN(mx[0]); PIN(mx[1]); PIN(mx[2]); PIN(mx[3]);
            f32x4 o[4], l[4];
#pragma unroll
            for (int m = 0; m < 4; ++m) {
#pragma unroll
                for (int j = 0; j < 4; ++j) { sp[m][j] = __builtin_amdgcn_exp2f(sp[m][j] - mx[m]); sc[m][j] = __builtin_amdgcn_exp2f(sc[m][j] - mx[m]); }
                const u32x2 pp = pack4(sp[m]), pc2 = pack4(sc[m]);
                const bf16x8 pb = __builtin_bit_cast(bf16x8, u32x4{pp[0], pp[1], pc2[0], pc2[1]});
                const bf16x8 va = __builtin_bit_cast(bf16x8, u32x4{vp[m][0], vp[m][1], vp[m + 1][0], vp[m + 1][1]});
                o[m] = MF16(va, pb, zero); l[m] = MF16(ones, pb, zero);
            }
#pragma unroll
            for (int m = 0; m < 4; ++m) op[m] = pack4(o[m] * __builtin_amdgcn_rcpf(l[m][0]));
#pragma unroll
            for (int i = 0; i < 2; ++i) st16(ws + WS_O + (size_t)(rt * 2 + i) * 65536 + h1 * 1024 + l16, join2(op[2 * i], op[2 * i + 1]));
        }
        STAMP(3);
    }

    {
        const int r32 = lane & 31, hh = lane >> 5;
        const int qt = qb * 8 + wid;
        const int vl = hh * 512 + (r32 & 15) * 16;
        f32x16 o; bf16x8 qf;
        {
            float o0, o1, o2, o3, o4, o5, o6, o7, o8;
            unsigned kpa = lds0 + L_KV + l16, vpa = lds0 + (r32 < 16 ? L_KV + 8192 : L_ONES) + vl, cnt = 8;
            const unsigned qa = lds0 + L_Q + wid * 1024 + l16, vstep = r32 < 16 ? 16384 : 0;
#define XE(b, i) "v_exp_f32 v[" #b "+" #i "], v[" #b "+" #i "]\n\t"
#define ASTEP(cur, nxt, uv, uk, c4, n4) \
            XE(cur, 0) XE(cur, 1) \
            "v_smfmac_f32_32x32x32_bf16 v[64:79], v[112+" #n4 ":115+" #n4 "], v[" #nxt "+8:" #nxt "+15], v120\n\t" \
            "ds_read_b128 v[104+" #n4 ":107+" #n4 "], %[vp] offset:" #uv "*1024\n\t" \
            "ds_read_b128 v[112+" #n4 ":115+" #n4 "], %[vp] offset:" #uv "*1024+256\n\t" \
            XE(cur, 2) XE(cur, 3) XE(cur, 4) XE(cur, 5) \
            "s_waitcnt lgkmcnt(2)\n\t" \
            "v_mfma_f32_32x32x16_bf16 v[" #nxt ":" #nxt "+15], v[96+" #n4 ":99+" #n4 "], %[q], v[80:95]\n\t" \
            "ds_read_b128 v[96+" #c4 ":99+" #c4 "], %[kp] offset:" #uk "*1024\n\t" \
            XE(cur, 6) XE(cur, 7) XE(cur, 8) XE(cur, 9) XE(cur, 10) XE(cur, 11) \
            "v_smfmac_f32_32x32x32_bf16 v[64:79], v[104+" #c4 ":107+" #c4 "], v[" #cur ":" #cur "+7], v120\n\t" \
            XE(cur, 12) XE(cur, 13) XE(cur, 14) XE(cur, 15)
#define XM(r, v) "v_mov_b32 v" #r ", " v "\n\t"
#define XM4(a, b, c, d, v) XM(a, v) XM(b, v) XM(c, v) XM(d, v)
#define XX3(a, b) "v_maximum3_f32 v121, v121, v" #a ", v" #b "\n\t"
            asm volatile(
                "s_waitcnt lgkmcnt(0)\n\t"
                "ds_read_b128 %[q], %[qa]\n\t"
                "ds_read_b128 v[96:99], %[kp]\n\t"
                "ds_read_b128 v[104:107], %[vp]\n\t"
                "ds_read_b128 v[112:115], %[vp] offset:256\n\t"
                "ds_read_b128 v[100:103], %[kp] offset:1024\n\t"
                XM4(64, 65, 66, 67, "0") XM4(68, 69, 70, 71, "0") XM4(72, 73, 74, 75, "0") XM4(76, 77, 78, 79, "0")
                "s_waitcnt lgkmcnt(3)\n\t"
                "v_mfma_f32_32x32x16_bf16 v[32:47], v[96:99], %[q], 0\n\t"
                XM4(56, 57, 58, 59, "0") XM4(60, 61, 62, 63, "0") XM4(116, 117, 118, 119, "0") XM(120, "0xdddd")
                "v_maximum3_f32 v121, v32, v33, v34\n\t"
                XX3(35, 36) XX3(37, 38) XX3(39, 40) XX3(41, 42) XX3(43, 44) XX3(45, 46) XX3(47, 47)
                "v_mov_b32 v122, v121\n\t"
                "s_nop 1\n\t"
                "v_permlane32_swap_b32 v121, v122\n\t"
                "v_maximum3_f32 v121, v121, v122, v122\n\t"
                "v_sub_f32 v80, 0, v121\n\t"
                XM(81, "v80") XM(82, "v80") XM(83, "v80") XM4(84, 85, 86, 87, "v80") XM4(88, 89, 90, 91, "v80") XM4(92, 93, 94, 95, "v80")
                "s_nop 1\n\t"
                "v_mfma_f32_32x32x16_bf16 v[32:47], v[96:99], %[q], v[80:95]\n\t"
                "s_nop 11\n\t"
                "1:\n\t"
                ASTEP(32, 48, 1, 2, 0, 4) ASTEP(48, 32, 2, 3, 4, 0) ASTEP(32, 48, 3, 4, 0, 4) ASTEP(48, 32, 4, 5, 4, 0) ASTEP(32, 48, 5, 6, 0, 4) ASTEP(48, 32, 6, 7, 4, 0)
                "s_waitcnt vmcnt(0)\n\t"
                "s_barrier\n\t"
                "v_add_u32 %[kp], 0x4000, %[kp]\n\t"
                ASTEP(32, 48, 7, 0, 0, 4)
                "v_add_u32 %[vp], %[vs], %[vp]\n\t"
                ASTEP(48, 32, 0, 1, 4, 0)
                "s_sub_u32 %[cnt], %[cnt], 1\n\t"
                "s_cmp_lg_u32 %[cnt], 0\n\t"
                "s_cbranch_scc1 1b\n\t"
                "v_smfmac_f32_32x32x32_bf16 v[64:79], v[116:119], v[56:63], v120\n\t"
                "s_waitcnt lgkmcnt(0)\n\t"
                "s_nop 10\n\t"
                "v_mov_b32 %[o0], v64\n\tv_mov_b32 %[o1], v65\n\tv_mov_b32 %[o2], v66\n\tv_mov_b32 %[o3], v67\n\t"
                "v_mov_b32 %[o4], v68\n\tv_mov_b32 %[o5], v69\n\tv_mov_b32 %[o6], v70\n\tv_mov_b32 %[o7], v71\n\tv_mov_b32 %[o8], v72\n\t"
                : [o0] "=&v"(o0), [o1] "=&v"(o1), [o2] "=&v"(o2), [o3] "=&v"(o3), [o4] "=&v"(o4), [o5] "=&v"(o5), [o6] "=&v"(o6), [o7] "=&v"(o7), [o8] "=&v"(o8),
                  [q] "=&v"(qf), [kp] "+v"(kpa), [vp] "+v"(vpa), [cnt] "+s"(cnt)
                : [qa] "v"(qa), [vs] "v"(vstep)
                : "memory", "scc",
                  "v32", "v33", "v34", "v35", "v36", "v37", "v38", "v39", "v40", "v41", "v42", "v43", "v44", "v45", "v46", "v47",
                  "v48", "v49", "v50", "v51", "v52", "v53", "v54", "v55", "v56", "v57", "v58", "v59", "v60", "v61", "v62", "v63",
                  "v64", "v65", "v66", "v67", "v68", "v69", "v70", "v71", "v72", "v73", "v74", "v75", "v76", "v77", "v78", "v79",
                  "v80", "v81", "v82", "v83", "v84", "v85", "v86", "v87", "v88", "v89", "v90", "v91", "v92", "v93", "v94", "v95",
                  "v96", "v97", "v98", "v99", "v100", "v101", "v102", "v103", "v104", "v105", "v106", "v107", "v108", "v109", "v110", "v111",
                  "v112", "v113", "v114", "v115", "v116", "v117", "v118", "v119", "v120", "v121", "v122");
            o[0] = o0; o[1] = o1; o[2] = o2; o[3] = o3; o[4] = o4; o[5] = o5; o[6] = o6; o[7] = o7; o[8] = o8;
        }
        STAMP(4);
        {
            float chk = MX(MX(o[8], fabsf(o[0])), fabsf(o[1]));
#pragma unroll
            for (int r = 2; r < 8; r += 2) chk = MX(MX(chk, fabsf(o[r])), fabsf(o[r + 1]));
            if (__any(!(chk < 1e30f))) {
                const __amdgpu_buffer_rsrc_t rs = __builtin_amdgcn_make_buffer_rsrc(ws + WS_K + hB, 0, (2u << 20) + 65536, 0x00020000);
                const u32x4 ones = {0x3f803f80u, 0x3f803f80u, 0x3f803f80u, 0x3f803f80u};
                float m = -INFINITY; o = f32x16{};
#pragma unroll 1
                for (int t = 0; t < 64; ++t) {
                    const u32x4 kf = __builtin_amdgcn_raw_buffer_load_b128(rs, l16, t * 1024, 16);
                    u32x4 v0 = __builtin_amdgcn_raw_buffer_load_b128(rs, vl, (2u << 20) + t * 1024, 16);
                    u32x4 v1 = __builtin_amdgcn_raw_buffer_load_b128(rs, vl + 256, (2u << 20) + t * 1024, 16);
                    if (r32 >= 16) { v0 = ones; v1 = ones; }
                    f32x16 z = {}; f32x16 s = MF32(BF8(kf), qf, z);
                    float tm = s[0];
#pragma unroll
                    for (int r = 1; r < 16; ++r) tm = MX(tm, s[r]);
                    const float mn = MX(m, halfmax(tm));
                    const float al = __builtin_amdgcn_exp2f(m - mn); m = mn;
#pragma unroll
                    for (int r = 0; r < 16; ++r) { s[r] = __builtin_amdgcn_exp2f(s[r] - mn); o[r] *= al; }
                    o = SMF(BF8(v0), LO8(s), o); o = SMF(BF8(v1), HI8(s), o);
                }
            }
        }
        {
            const float sc = __builtin_amdgcn_rcpf(o[8]);
            const unsigned a0 = cvtpk(o[0] * sc, o[1] * sc), a1 = cvtpk(o[2] * sc, o[3] * sc);
            const unsigned b0 = cvtpk(o[4] * sc, o[5] * sc), b1 = cvtpk(o[6] * sc, o[7] * sc);
            auto r0 = __builtin_amdgcn_permlane32_swap(a0, b0, false, false);
            auto r1 = __builtin_amdgcn_permlane32_swap(a1, b1, false, false);
            st16(ws + WS_O + (size_t)qt * 65536 + (NH + head) * 1024 + l16, u32x4{r0[0], r1[0], r0[1], r1[1]});
        }
        publish(lds, L_CTL + 20, 7, ws + WS_F2 + (qb * 32 + head) * FS, lane);
        STAMP(5);
        BAR();
    }

    {
        const int rl = (wid >> 1) * 16 + fr;
        const lds_cptr op = (lds_cptr)lds + L_KV + 65536 + (wid >> 2) * 32768 + fg * 512 + (((wid >> 1) & 1) * 16 + fr) * 16;
        const lds_cptr wp = (lds_cptr)lds + L_KV + (wid & 1) * 32768 + fg * 512 + fr * 16;
        const u32x2 pa0 = *(const LAS u32x2*)((lds_cptr)lds + L_PA + rl * PA_LD + (wid & 1) * 64 + fg * 8);
        const u32x2 pa1 = *(const LAS u32x2*)((lds_cptr)lds + L_PA + rl * PA_LD + (wid & 1) * 64 + 32 + fg * 8);
        const float g1 = bf16r(1.f - *(const LAS float*)((lds_cptr)lds + L_GT + rl * 4));
        f32x4 d0 = {}, d1 = {};
#pragma unroll
        for (int st = 0; st < 4; ++st) {
            BAR();
            if (st == 0) STAMP(6); if (st == 3) STAMP(7);
#pragma unroll
            for (int k = 0; k < 4; ++k) {
                const int ko = (k * 4 + st) * 2048;
                const bf16x8 of = lds_ld16(op + ko), w0 = lds_ld16(wp + ko), w1 = lds_ld16(wp + ko + 256);
                d0 = MF16(w0, of, d0); d1 = MF16(w1, of, d1);
            }
        }
        f32x4 r0, r1;
#pragma unroll
        for (int i = 0; i < 2; ++i) {
            r0[2 * i] = bflo(pa0[i]) + bf16r(g1 * bf16r(d0[2 * i])); r0[2 * i + 1] = bfhi(pa0[i]) + bf16r(g1 * bf16r(d0[2 * i + 1]));
            r1[2 * i] = bflo(pa1[i]) + bf16r(g1 * bf16r(d1[2 * i])); r1[2 * i + 1] = bfhi(pa1[i]) + bf16r(g1 * bf16r(d1[2 * i + 1]));
        }
        *(u32x4*)((char*)out + (size_t)(r64 * 64 + rl) * 1024 + (nb * 64 + (wid & 1) * 32 + (fg & 1) * 16 + (fg >> 1) * 8) * 2) = join2(pack4(r0), pack4(r1));
    }
    STAMP(8); STAMP_FLUSH(ws, 0);
}

extern "C" void kernel_launch(void* const* d_in, const int* in_sizes, int n_in, void* d_out, int out_size, void* d_ws,
                              size_t ws_size, hipStream_t stream) {
    (void)hipFuncSetAttribute((const void*)fused, hipFuncAttributeMaxDynamicSharedMemorySize, LDS_BYTES);
    fused<<<256, 768, LDS_BYTES, stream>>>((const __hip_bfloat16*)d_in[0], (const __hip_bfloat16*)d_in[1], (const __hip_bfloat16*)d_in[4],
                                           (const __hip_bfloat16*)d_in[2], (const __hip_bfloat16*)d_in[5], (const __hip_bfloat16*)d_in[6],
                                           (char*)d_ws, (__hip_bfloat16*)d_out);
}
```

```cpp
#include <hip/hip_runtime.h>
#include <hip/hip_bf16.h>
#include <stdint.h>

typedef __attribute__((ext_vector_type(8))) __bf16 bf16x8;
typedef __attribute__((ext_vector_type(16))) __bf16 bf16x16;
typedef __attribute__((ext_vector_type(4))) short s16x4;
typedef __attribute__((ext_vector_type(4))) float f32x4;
typedef __attribute__((ext_vector_type(16))) float f32x16;
typedef __attribute__((ext_vector_type(2))) unsigned u32x2;
typedef __attribute__((ext_vector_type(4))) unsigned u32x4;
typedef __attribute__((address_space(3))) const char* lds_cptr;
typedef __attribute__((address_space(3))) char* lds_ptr;

constexpr int NH = 32;
constexpr float QSCALE = 0.25f * 1.4426950408889634f;

constexpr size_t WS_Q = 0;
constexpr size_t WS_K = WS_Q + (2u << 20);
constexpr size_t WS_V = WS_K + (2u << 20);
constexpr size_t WS_O = WS_V + (2u << 20);
constexpr size_t WS_W = WS_O + (4u << 20);
constexpr size_t WS_G = WS_W + (1u << 20);
constexpr int F2S = 512;
constexpr int FS = 128;
constexpr size_t WS_FB = WS_G + 8192;
constexpr size_t WS_FA = WS_FB + 256 * FS;
constexpr size_t WS_F2 = WS_FA + 256 * FS;
constexpr size_t WS_E = WS_F2 + 256 * F2S;

#define PIN(x) asm volatile("" : "+v"(x))
#define PIN8(d) asm volatile("" : "+v"(d[0]), "+v"(d[1]), "+v"(d[2]), "+v"(d[3]), "+v"(d[4]), "+v"(d[5]), "+v"(d[6]), "+v"(d[7]))
#define SBAR() __builtin_amdgcn_sched_barrier(0)

__device__ __forceinline__ unsigned cvtpk(float lo, float hi) {
    typedef __attribute__((ext_vector_type(2))) float f32x2_; typedef __attribute__((ext_vector_type(2))) __bf16 bf16x2_;
    return __builtin_bit_cast(unsigned, __builtin_convertvector(f32x2_{lo, hi}, bf16x2_));
}
__device__ __forceinline__ float bf16r(float x) { return __uint_as_float(cvtpk(x, x) << 16); }
__device__ __forceinline__ float bflo(unsigned u) { return __uint_as_float(u << 16); }
__device__ __forceinline__ float bfhi(unsigned u) { return __uint_as_float(u & 0xffff0000u); }
__device__ __forceinline__ void glds16s(const void* sbase, unsigned voff, unsigned lds_base) {
    asm volatile("s_mov_b32 m0, %2\n\ts_nop 0\n\tglobal_load_lds_dwordx4 %0, %1" :: "v"(voff), "s"(sbase), "s"(lds_base) : "memory");
}
__device__ __forceinline__ void glds4v(const void* vaddr, unsigned lds_base) {
    asm volatile("s_mov_b32 m0, %1\n\ts_nop 0\n\tglobal_load_lds_dword %0, off sc1" :: "v"(vaddr), "s"(lds_base) : "memory");
}
__device__ __forceinline__ void glds16d(const void* sbase, unsigned voff, unsigned lds_base) {
    asm volatile("s_mov_b32 m0, %2\n\ts_nop 0\n\tglobal_load_lds_dwordx4 %0, %1 sc1" :: "v"(voff), "s"(sbase), "s"(lds_base) : "memory");
}
__device__ __forceinline__ void st16(void* p, u32x4 v) {
    asm volatile("global_store_dwordx4 %0, %1, off sc1\n\ts_nop 1" :: "v"(p), "v"(v) : "memory");
}
typedef __attribute__((address_space(1))) unsigned long long gu64;
typedef __attribute__((address_space(1))) unsigned gu32;
#define RLX_AGENT __ATOMIC_RELAXED, __HIP_MEMORY_SCOPE_AGENT
#define LAS __attribute__((address_space(3)))
__device__ __forceinline__ void after_poll() { __builtin_amdgcn_fence(__ATOMIC_ACQUIRE, "wavefront"); }
constexpr int CODE_PF = 24320;
constexpr unsigned SPIN_CAP = 1u << 17;
__device__ __forceinline__ s16x4 lds_tr(lds_cptr p) {
    return __builtin_amdgcn_ds_read_tr16_b64_v4i16((__attribute__((address_space(3))) s16x4*)p);
}

constexpr int XSLAB = 10240, L_W = 8 * XSLAB, WSLAB = 24576, L_GW = L_W + 3 * WSLAB, L_CTL = L_GW + 2048, L_WO = L_CTL + 128, L_PL = L_WO + 4096, LDS_BYTES = L_PL + 1024;
constexpr int L_Q = 0, L_KV = 8192, L_PA = L_KV + 8 * 16384, PA_LD = 144, L_GT = L_PA + 64 * PA_LD, L_X3 = L_CTL - 8192;
static_assert(L_GT + 256 <= L_X3 && L_KV + 2 * 16384 <= 5 * XSLAB, "LDS map");
__device__ __forceinline__ void lds_wait(const char* lds, int off, unsigned v) {
    while ((unsigned)__builtin_amdgcn_readfirstlane(*(const volatile LAS unsigned*)((lds_cptr)lds + off)) < v) __builtin_amdgcn_s_sleep(1);
    asm volatile("" ::: "memory");
}
__device__ __forceinline__ void lds_inc(const char* lds, int off) {
    asm volatile("s_mov_b64 exec, 1\n\tds_add_u32 %0, %1\n\ts_mov_b64 exec, -1" :: "v"((unsigned)(uintptr_t)lds + off), "v"(1u) : "memory");
}
#define MF16(a, b, c) __builtin_amdgcn_mfma_f32_16x16x32_bf16(a, b, c, 0, 0, 0)
#define MF16K16(a, b, c) __builtin_amdgcn_mfma_f32_16x16x16bf16_1k(__builtin_bit_cast(s16x4, a), __builtin_bit_cast(s16x4, b), c, 0, 0, 0)
#define BAR_L() asm volatile("s_waitcnt lgkmcnt(0)\n\ts_barrier" ::: "memory")

__device__ __forceinline__ u32x2 pack4(const f32x4& a) { return u32x2{cvtpk(a[0], a[1]), cvtpk(a[2], a[3])}; }
__device__ __forceinline__ u32x4 join2(u32x2 a, u32x2 b) {
    auto r0 = __builtin_amdgcn_permlane16_swap(a[0], b[0], false, false);
    auto r1 = __builtin_amdgcn_permlane16_swap(a[1], b[1], false, false);
    return u32x4{r0[0], r1[0], r0[1], r1[1]};
}
#define MX(a, b) __builtin_elementwise_maximum(a, b)
#define FU(x) __float_as_uint(x)
#define UF(x) __uint_as_float(x)
__device__ __forceinline__ void xmax4x4(float (&m)[4]) {
    auto a = __builtin_amdgcn_permlane32_swap(FU(m[0]), FU(m[1]), false, false);
    auto b = __builtin_amdgcn_permlane32_swap(FU(m[2]), FU(m[3]), false, false);
    auto c = __builtin_amdgcn_permlane16_swap(FU(MX(UF(a[0]), UF(a[1]))), FU(MX(UF(b[0]), UF(b[1]))), false, false);
    const unsigned y = FU(MX(UF(c[0]), UF(c[1])));
    auto d = __builtin_amdgcn_permlane16_swap(y, y, false, false);
    auto e = __builtin_amdgcn_permlane32_swap(d[0], d[0], false, false);
    auto f = __builtin_amdgcn_permlane32_swap(d[1], d[1], false, false);
    m[0] = UF(e[0]); m[1] = UF(e[1]); m[2] = UF(f[0]); m[3] = UF(f[1]);
}

#define BF8(v) __builtin_bit_cast(bf16x8, v)
template <bool HALO, bool TAILBAR>
__device__ __forceinline__ void proj_pass(lds_cptr lds, int xo, int wo, int wsl, f32x4 (&aq)[4], f32x4 (&ak)[5], f32x4 (&av)[5]) {
    constexpr int M0 = HALO ? 0 : 1, NRD = 6 + 5 - M0, NMF = 12 + (HALO ? 2 : 0);
    u32x4 xa[5], wa[3], xb[5], wb[3];
    auto ld = [&](u32x4 (&xf)[5], u32x4 (&wf)[3], int xs, int kk) {
        xf[1] = *(const LAS u32x4*)(lds + xs + (xo ^ (kk * 64)) + 2048);
#pragma unroll
        for (int s = 0; s < 3; ++s) {
            const u32x2 lo = __builtin_bit_cast(u32x2, lds_tr(lds + wsl + wo + s * 8192 + kk * 4096));
            const u32x2 hi = __builtin_bit_cast(u32x2, lds_tr(lds + wsl + wo + s * 8192 + kk * 4096 + 512));
            wf[s] = u32x4{lo[0], lo[1], hi[0], hi[1]};
        }
#pragma unroll
        for (int m = 2; m < 5; ++m) xf[m] = *(const LAS u32x4*)(lds + xs + (xo ^ (kk * 64)) + m * 2048);
        if (HALO) xf[0] = *(const LAS u32x4*)(lds + xs + (xo ^ (kk * 64)));
    };
    auto mm1 = [&](u32x4 (&xf)[5], u32x4 (&wf)[3], int m) {
        if (m) aq[m - 1] = MF16(BF8(wf[0]), BF8(xf[m]), aq[m - 1]);
        ak[m] = MF16(BF8(wf[1]), BF8(xf[m]), ak[m]);
        av[m] = MF16(BF8(xf[m]), BF8(wf[2]), av[m]);
    };
    auto rest = [&](u32x4 (&xf)[5], u32x4 (&wf)[3]) { mm1(xf, wf, 2); mm1(xf, wf, 3); mm1(xf, wf, 4); if (HALO) mm1(xf, wf, 0); };
    auto weave = [&]() {
        __builtin_amdgcn_sched_group_barrier(0x100, NRD - (NMF - 3) + 1, 0);
#pragma unroll
        for (int i = 0; i < NMF - 4; ++i) { __builtin_amdgcn_sched_group_barrier(0x8, 1, 0); __builtin_amdgcn_sched_group_barrier(0x100, 1, 0); }
        __builtin_amdgcn_sched_group_barrier(0x8, 1, 0);
    };
#define WBAR() do { __builtin_amdgcn_s_waitcnt(0xC07F); __builtin_amdgcn_s_barrier(); asm volatile("" ::: "memory"); SBAR(); } while (0)
    WBAR();
    ld(xa, wa, 0, 0); SBAR();
#pragma unroll 1
    for (int t = 0; t < 7; ++t) {
        mm1(xa, wa, 1); SBAR();
        __builtin_amdgcn_s_waitcnt(0xC07F); SBAR();
        ld(xb, wb, t * XSLAB, 1); rest(xa, wa); weave(); SBAR();
        wsl = wsl == 2 * WSLAB ? 0 : wsl + WSLAB;
        mm1(xb, wb, 1); SBAR(); WBAR();
        ld(xa, wa, (t + 1) * XSLAB, 0); rest(xb, wb); weave(); SBAR();
    }
    mm1(xa, wa, 1); SBAR();
    __builtin_amdgcn_s_waitcnt(0xC07F); SBAR();
    ld(xb, wb, 7 * XSLAB, 1); rest(xa, wa); weave(); SBAR();
    mm1(xb, wb, 1); SBAR();
    if (TAILBAR) WBAR();
    rest(xb, wb);
}

#define SMF(a, plo, acc) __builtin_amdgcn_smfmac_f32_32x32x32_bf16(a, __builtin_bit_cast(bf16x16, plo), acc, 0xDDDD, 0, 0)
#define LO8(p) __builtin_shufflevector(p, p, 0, 1, 2, 3, 4, 5, 6, 7)
#define HI8(p) __builtin_shufflevector(p, p, 8, 9, 10, 11, 12, 13, 14, 15)
#define MF32(a, b, c) __builtin_amdgcn_mfma_f32_32x32x16_bf16(a, b, c, 0, 0, 0)
#define BAR() asm volatile("s_barrier" ::: "memory")

__device__ __forceinline__ float halfmax(float m) {
    auto rr = __builtin_amdgcn_permlane32_swap(FU(m), FU(m), false, false);
    return MX(UF(rr[0]), UF(rr[1]));
}
__device__ __forceinline__ void publish(char* lds, int cnt_off, unsigned last, char* flag, int lane) {
    asm volatile("s_waitcnt vmcnt(0)" ::: "memory");
    if (lane == 0) {
        const unsigned old = __hip_atomic_fetch_add((LAS unsigned*)((lds_ptr)lds + cnt_off), 1u, __ATOMIC_RELAXED, __HIP_MEMORY_SCOPE_WORKGROUP);
        if (old == last) __hip_atomic_store((gu64*)flag, *(volatile LAS unsigned long long*)((lds_ptr)lds + L_CTL), RLX_AGENT);
    }
}

__global__ __launch_bounds__(768, 3) void fused(const __hip_bfloat16* __restrict__ x, const __hip_bfloat16* __restrict__ wA,
                                                const __hip_bfloat16* __restrict__ wB, const __hip_bfloat16* __restrict__ woA,
                                                const __hip_bfloat16* __restrict__ woB, const __hip_bfloat16* __restrict__ gw,
                                                char* ws, __hip_bfloat16* __restrict__ out) {
    extern __shared__ __attribute__((aligned(16))) char lds[];
    unsigned long long pcv; asm volatile("s_getpc_b64 %0" : "=s"(pcv));
    const int tid = threadIdx.x, lane = tid & 63;
    const int wid = __builtin_amdgcn_readfirstlane(tid >> 6);
    const int b = blockIdx.x;
    const int hg = (b & 6) | ((b >> 3) & 1), rt = (b & 1) * 16 + (b >> 4);
    const int head = (b & 7) * 4 + ((b >> 3) & 3), qb = b >> 5;
    const int r64 = head, nb = qb;
    const unsigned lds0 = (unsigned)(uintptr_t)lds, l16 = lane * 16;
    const size_t hB = (size_t)head * 65536;

    const int pr = lane >> 3, pc = lane & 7;
    const int rot = hg + rt;
    if (__builtin_expect(wid >= 4, 1)) {
        const int j = wid - 4;
        const unsigned vw = pr * 3072 + (((pc >> 1) ^ (((pr >> 1) & 1) | ((j & 1) << 1))) << 5) + ((pc & 1) << 4);
        const unsigned vx = pr * 1024 + ((pc ^ ((4 * (j & 1) + (pr >> 1)) & 7)) << 4);
        const char* xg = (const char*)x + (size_t)(rt * 64 + j * 8) * 1024;
        const char* wg = (const char*)wB + hg * 128 + j * 8 * 3072;
        int so = 0;
#pragma unroll 1
        for (int t = 0; t < 8; ++t) {
            if (t >= 2) asm volatile("s_waitcnt vmcnt(4)\n\ts_barrier" ::: "memory");
            const int kt = (t + rot) & 7;
#pragma unroll
            for (int i = 0; i < 3; ++i) glds16s(wg + (size_t)kt * 64 * 3072 + i * 1024, vw, lds0 + L_W + so + i * 8192 + j * 1024);
            glds16s(xg + kt * 128, vx, lds0 + t * XSLAB + 2048 + j * 1024);
            so = so == 2 * WSLAB ? 0 : so + WSLAB;
        }
    } else {
        if (wid == 0) {
            glds4v(lane < 2 ? ws + WS_E + lane * 4 : (const char*)(pcv & ~255ull) + min(((b >> 3) + (lane - 2) * 32) * 128, CODE_PF), lds0 + L_GW + 1024);
            glds16s(gw, l16, lds0 + L_GW);
            if (lane < 8) *(LAS unsigned*)((lds_ptr)lds + L_CTL + 8 + lane * 4) = 0u;
        }
        if (wid) {
            const int j = b >> 4, ord = ((j >> 1) - rot) & 7;
            const size_t po = (size_t)(j * 32 + (lane & 31)) * 3072 + (wid - 1) * 1024 + hg * 128;
            for (int i = 0; i < ord * 2; ++i) __builtin_amdgcn_s_sleep(1);
            for (int i = 0; i < 8; ++i) __builtin_amdgcn_s_sleep(1);
            glds4v((const char*)wA + po, lds0 + L_PL + wid * 256);
        }
        glds16s((const char*)(b < 128 ? woA : woB) + (((b >> 2) & 31) * 16 + wid * 4) * 1024 + (b & 3) * 256, (lane >> 4) * 1024 + (lane & 15) * 16, lds0 + L_WO + wid * 1024);
    }
    if (wid >= 8) {
        const int hw = wid - 8;
        __builtin_amdgcn_s_setprio(3);
        const char* fkv = ws + WS_FB + (head >> 2) * 32 * FS;
        {
            unsigned vw[2];
#pragma unroll
            for (int p = 0; p < 2; ++p) vw[p] = pr * 3072 + (((pc >> 1) ^ (((pr >> 1) & 1) | (p << 1))) << 5) + ((pc & 1) << 4);
            const unsigned vh = pr * 1024 + ((pc ^ ((4 * (hw & 1) + (pr >> 1)) & 7)) << 4);
            const char* xh = (const char*)x + (size_t)((rt ? rt * 64 - 16 : 0) + (hw & 1) * 8) * 1024;
            const unsigned lw = lds0 + L_W + hw * 2048;
            int so = 2 * WSLAB;
#pragma unroll 1
            for (int t = 0; t < 8; ++t) {
                if (!t) asm volatile("s_waitcnt vmcnt(4)" ::: "memory");
                BAR();
                const int kt = (t + rot) & 7;
                const char* wg = (const char*)wA + hg * 128 + (size_t)(kt * 64 + hw * 16) * 3072;
                if (hw < 2) glds16s(xh + kt * 128, vh, lds0 + t * XSLAB + hw * 1024);
#pragma unroll
                for (int i = 0; i < 6; ++i) glds16s(wg + (i >> 1) * 1024 + (i & 1) * 8 * 3072, vw[i & 1], lw + so + (i >> 1) * 8192 + (i & 1) * 1024);
                so = so == 2 * WSLAB ? 0 : so + WSLAB;
                if (hw < 2) asm volatile("s_waitcnt vmcnt(7)" ::: "memory"); else asm volatile("s_waitcnt vmcnt(6)" ::: "memory");
            }
            asm volatile("s_barrier\n\ts_waitcnt vmcnt(0)\n\ts_barrier" ::: "memory");
        }
        const unsigned long long tag = (unsigned)__builtin_amdgcn_readfirstlane(*(volatile LAS unsigned*)((lds_ptr)lds + L_CTL))
                                     | (unsigned long long)__builtin_amdgcn_readfirstlane(*(volatile LAS unsigned*)((lds_ptr)lds + L_CTL + 4)) << 32;
        unsigned spins = 0, ready = 0;
        auto quads = [](unsigned long long bal) { unsigned v = (unsigned)bal; v &= v >> 1; v &= v >> 2; unsigned r = 0;
#pragma unroll
            for (int c = 0; c < 8; ++c) r |= ((v >> (4 * c)) & 1u) << c;
            return r; };
        volatile LAS unsigned* seen = (volatile LAS unsigned*)((lds_ptr)lds + L_CTL + 16);
        auto await = [&](unsigned m, const char* fbase, int nl, bool kv) {
            if ((ready & m) == m) return;
            if (hw == 3) {
                for (;;) {
                    bool ok = __hip_atomic_load((gu64*)(fbase + (lane & (nl - 1)) * FS), RLX_AGENT) == tag;
                    if (nl == 256) {
#pragma unroll
                        for (int i = 1; i < 4; ++i) ok &= __hip_atomic_load((gu64*)(fbase + (i * 64 + lane) * FS), RLX_AGENT) == tag;
                    }
                    const unsigned long long bal = __ballot(ok);
                    ready |= kv ? quads(bal) : (bal == ~0ull ? m : 0u);
                    if ((ready & m) == m) break;
                    if (++spins > SPIN_CAP) { ready = ~0u; break; }
                    __builtin_amdgcn_s_sleep(1);
                }
                *seen = ready;
            } else {
                for (;;) { ready |= __builtin_amdgcn_readfirstlane(*seen); if ((ready & m) == m) break; __builtin_amdgcn_s_sleep(1); }
            }
            after_poll();
        };
        const char* ksrc = ws + WS_K + hB + hw * 2048;
        const char* vsrc = ws + WS_V + hB + hw * 2048;
        const unsigned ldsw = lds0 + L_KV + hw * 2048;
        auto chunk = [&](int s) {
            const int c = (qb + s) & 7;
            await(1u << c, fkv, 32, true);
            glds16d(ksrc + c * 8192, l16, ldsw + s * 16384); glds16d(vsrc + c * 8192, l16, ldsw + 8192 + s * 16384);
            glds16d(ksrc + c * 8192 + 1024, l16, ldsw + s * 16384 + 1024); glds16d(vsrc + c * 8192 + 1024, l16, ldsw + 8192 + s * 16384 + 1024);
        };
        chunk(1);
        const int r32 = lane & 31, hh = lane >> 5, n32 = nb * 2 + (hw & 1);
        const char* wsrc = ws + WS_W + n32 * 65536 + 4096;
        u32x4 w0[8], w1[8], w2[8], w3[8];
#define LD8(d, base) asm volatile( \
            "global_load_dwordx4 %0, %8, %9 offset:-4096 sc1\n\tglobal_load_dwordx4 %1, %8, %9 offset:-3072 sc1\n\t" \
            "global_load_dwordx4 %2, %8, %9 offset:-2048 sc1\n\tglobal_load_dwordx4 %3, %8, %9 offset:-1024 sc1\n\t" \
            "global_load_dwordx4 %4, %8, %9 sc1\n\tglobal_load_dwordx4 %5, %8, %9 offset:1024 sc1\n\t" \
            "global_load_dwordx4 %6, %8, %9 offset:2048 sc1\n\tglobal_load_dwordx4 %7, %8, %9 offset:3072 sc1" \
            : "=&v"(d[0]), "=&v"(d[1]), "=&v"(d[2]), "=&v"(d[3]), "=&v"(d[4]), "=&v"(d[5]), "=&v"(d[6]), "=&v"(d[7]) : "v"(l16), "s"(base) : "memory")
        asm volatile("s_waitcnt vmcnt(0)\n\ts_barrier" ::: "memory");
        if (tid == 704) __hip_atomic_store((gu64*)(ws + WS_FA + b * FS), tag, RLX_AGENT);
        chunk(2);
        asm volatile("s_waitcnt vmcnt(0)\n\ts_barrier" ::: "memory");
        chunk(3);
        await(0x100u, ws + WS_FA, 256, false);
        ready |= 0xffu;
        if (b == 0 && tid == 512) __hip_atomic_store((gu64*)(ws + WS_E), tag, RLX_AGENT);
        if (hw == 0) glds4v(ws + WS_G + (r64 * 64 + lane) * 4, lds0 + L_GT);
#define VBAR() asm volatile("s_waitcnt vmcnt(0)\n\ts_barrier" ::: "memory")
        auto run = [&](const char* src, unsigned dst, int n) {
#pragma unroll 1
            for (int i = 0; i < n; ++i) glds16d(src + i * 1024, l16, dst + i * 1024);
        };
        const char* osrc = ws + WS_O + (size_t)(r64 * 2) * 65536 + hw * 8192;
        LD8(w0, wsrc);
        VBAR(); chunk(4); LD8(w1, wsrc + 8192);
        VBAR(); chunk(5); LD8(w2, wsrc + 16384);
        VBAR(); chunk(6); LD8(w3, wsrc + 24576);
        VBAR(); chunk(7);
        run(ws + WS_W + (nb * 2 + (hw >> 1)) * 65536 + (32 + (hw & 1) * 16) * 1024, lds0 + L_KV + hw * 16384, 16);
        asm volatile("s_waitcnt vmcnt(16)\n\ts_barrier" ::: "memory");
        run(osrc, lds0 + L_KV + 65536 + hw * 8192, 8);
        if (hw >= 2) run(osrc + 65536, lds0 + (hw == 2 ? L_Q : L_X3), 8);
        VBAR();
        if (hw < 2) run(osrc + 65536, lds0 + L_KV + 98304 + hw * 8192, 8);
        PIN8(w0); PIN8(w1); PIN8(w2); PIN8(w3);
        auto project_a = [&] {
            f32x16 acc = {};
            const lds_cptr o01 = (lds_cptr)lds + L_KV + 65536 + (hw >> 1) * 32768 + l16;
            const lds_cptr o2 = (hw >> 1) ? (lds_cptr)lds + L_Q + l16 : o01 + 16384, o3 = (hw >> 1) ? (lds_cptr)lds + L_X3 + l16 : o01 + 24576;
            u32x4 f[32];
            auto rd = [&](int k) { f[k] = *(const LAS u32x4*)((k < 8 ? o2 : k < 16 ? o3 : o01 + ((k - 16) >> 3) * 8192) + (k & 7) * 1024); };
            rd(0); rd(1); rd(2); SBAR();
#pragma unroll
            for (int k = 0; k < 32; ++k) {
                const u32x4 w = k < 8 ? w2[k & 7] : k < 16 ? w3[k & 7] : k < 24 ? w0[k & 7] : w1[k & 7];
                acc = MF32(BF8(w), BF8(f[k]), acc);
                if (k == 8) {
                    unsigned sv, tv;
                    asm volatile("s_waitcnt vmcnt(0)\n\ts_mov_b64 exec, 1\n\tds_add_u32 %2, %3\n\ts_mov_b64 exec, -1\n\t"
                                 "1:\n\tds_read_b32 %0, %2\n\ts_waitcnt lgkmcnt(0)\n\tv_readfirstlane_b32 %1, %0\n\ts_cmp_lt_u32 %1, 4\n\ts_cbranch_scc1 1b"
                                 : "=&v"(tv), "=&s"(sv) : "v"(lds0 + L_CTL + 36), "v"(1u) : "memory", "scc");
                }
                if (k < 5) { rd(2 * k + 3); rd(2 * k + 4); } else if (k + 8 < 32) rd(k + 8);
                SBAR();
            }
            lds_ptr dst = (lds_ptr)lds + L_PA + ((hw >> 1) * 32 + r32) * PA_LD + (hw & 1) * 64 + hh * 8;
#pragma unroll
            for (int q = 0; q < 4; ++q)
                *(LAS u32x2*)(dst + q * 16) = u32x2{cvtpk(acc[4 * q], acc[4 * q + 1]), cvtpk(acc[4 * q + 2], acc[4 * q + 3])};
            lds_inc(lds, L_CTL + 32);
        };
        __builtin_amdgcn_s_setprio(0);
        project_a();
        __builtin_amdgcn_s_setprio(3);
        { const char* src = osrc + 32768;
          const char* fl = ws + WS_F2 + (r64 * 8 + nb) * F2S + (hw * 16 + (lane & 15)) * 8;
          const unsigned dst = lds0 + L_KV + hw * 8192;
          unsigned sent = 0;
#pragma unroll 1
          while (sent != 0xffffu) {
              bool hit = false;
              if (lane < 16 && !(sent >> lane & 1u)) hit = __hip_atomic_load((gu64*)fl, RLX_AGENT) == tag;
              unsigned bal = (unsigned)__ballot(hit);
              if (++spins > SPIN_CAP) bal = 0xffffu;
              after_poll();
              bal &= ~sent;
              if (!sent) lds_wait(lds, L_CTL + 28, 8);
              sent |= bal;
              if (!bal) __builtin_amdgcn_s_sleep(2);
#pragma unroll 1
              for (; bal; bal &= bal - 1) {
                  const unsigned i = __builtin_ctz(bal);
                  glds16d(src + (i & 1) * 65536 + (i >> 1) * 1024, l16, dst + (i & 1) * 32768 + (i >> 1) * 1024);
              }
          }
          asm volatile("s_waitcnt vmcnt(0)" ::: "memory");
          if (lane == 0) __hip_atomic_fetch_or((LAS unsigned*)((lds_ptr)lds + L_CTL + 12), 1u << hw, __ATOMIC_RELAXED, __HIP_MEMORY_SCOPE_WORKGROUP); }
        return;
    }

    const int fr = lane & 15, fg = lane >> 4;
    {
        const int hl = wid & 3, h1 = hg * 4 + hl;
        const int xo = fr * 128 + ((fg ^ ((fr >> 1) & 7)) << 4);
        int wo;
        { const int k = 8 * fg + (fr >> 2); const int f = ((k >> 1) & 1) | (((k >> 3) & 1) << 1);
          wo = L_W + k * 128 + ((hl ^ f) << 5) + ((fr & 3) << 3); }
        f32x4 aq[4] = {}, ak[5] = {}, av[5] = {};
        if (wid < 4) {
            const int ks = b >> 2, n32 = (b & 3) * 4 + wid;
            proj_pass<false, true>((lds_cptr)lds, xo, wo, 0, aq, ak, av);
            __builtin_amdgcn_s_setprio(3);
            asm volatile("s_waitcnt vmcnt(0)" ::: "memory");
            if (tid < 4) {
                const unsigned long long tg = *(const LAS unsigned long long*)((lds_cptr)lds + L_GW + 1024) + 0x9E3779B97F4A7C15ull;
                *(LAS unsigned long long*)((lds_ptr)lds + L_CTL) = tg; *(LAS unsigned long long*)((lds_ptr)lds + L_PL + tid * 8) = ~tg;
            }
            const int gr = 16 + hg * 8 + wid * 2;
            u32x4 gx0 = *(const LAS u32x4*)((lds_cptr)lds + pr * XSLAB + gr * 128 + ((pc ^ ((gr >> 1) & 7)) << 4));
            u32x4 gx1 = *(const LAS u32x4*)((lds_cptr)lds + pr * XSLAB + gr * 128 + 128 + ((pc ^ ((gr >> 1) & 7)) << 4));
            const size_t off = (size_t)h1 * 65536 + rt * 2048 + l16;
            st16(ws + WS_K + off, join2(pack4(ak[1]), pack4(ak[2]))); st16(ws + WS_K + off + 1024, join2(pack4(ak[3]), pack4(ak[4])));
            st16(ws + WS_V + off, join2(pack4(av[1]), pack4(av[2]))); st16(ws + WS_V + off + 1024, join2(pack4(av[3]), pack4(av[4])));
            st16(ws + WS_Q + off, join2(pack4(aq[0] * QSCALE), pack4(aq[1] * QSCALE)));
            st16(ws + WS_Q + off + 1024, join2(pack4(aq[2] * QSCALE), pack4(aq[3] * QSCALE)));
            BAR_L();
            const unsigned tagw = *(volatile LAS unsigned*)((lds_ptr)lds + L_CTL + (lane & 1) * 4);
            const char* fsrc = ws + WS_FB + ((head >> 2) * 32 + qb * 4) * FS;
            bool pub = false, got = false; unsigned spins = 0;
#pragma unroll 1
            for (int u = 10;;) {
                const bool idle = u >= 16 || (__builtin_amdgcn_s_getreg(63495) & 0xC0000Fu) == 0u;
                if (!pub && idle) { publish(lds, L_CTL + 8, 3, ws + WS_FB + (hg * 32 + rt) * FS, lane); pub = true; }
                if (!got && u > 11) {
                    if (u >= 16) { asm volatile("s_waitcnt vmcnt(0)" ::: "memory"); if (wid) __builtin_amdgcn_s_sleep(1); }
                    if (__all(*(volatile LAS unsigned*)((lds_ptr)lds + L_PL + (lane & 7) * 4) == tagw) || ++spins > SPIN_CAP * 16u) {
                        after_poll();
                        const size_t o2 = hB + qb * 8192 + wid * 2048;
#pragma unroll
                        for (int i = 0; i < 2; ++i) {
                            glds16d(ws + WS_K + o2 + i * 1024, l16, lds0 + L_KV + wid * 2048 + i * 1024);
                            glds16d(ws + WS_Q + o2 + i * 1024, l16, lds0 + L_Q + wid * 2048 + i * 1024);
                            glds16d(ws + WS_V + o2 + i * 1024, l16, lds0 + L_KV + 8192 + wid * 2048 + i * 1024);
                        }
                        got = true;
                    }
                }
                if (wid == 0 && pub && idle && !got && u >= 14 && lane < 8)
                    asm volatile("s_mov_b32 m0, %2\n\ts_nop 0\n\tglobal_load_lds_dword %0, %1 sc1" :: "v"((lane >> 1) * FS + (lane & 1) * 4), "s"(fsrc), "s"(lds0 + L_PL) : "memory");
                if (u >= 16) { if (got) break; }
                else { BAR(); ++u; }
            }
            PIN(gx0); PIN(gx1);
            lds_wait(lds, L_CTL + 8, 4);
            const u32x4 gv = *(const LAS u32x4*)((lds_cptr)lds + L_GW + ((pr + rot) & 7) * 128 + pc * 16);
            typedef __attribute__((ext_vector_type(2))) __bf16 bf16x2;
            float z = 0.f, z1 = 0.f;
#pragma unroll
            for (int i = 0; i < 4; ++i) {
                const unsigned x0 = gx0[i], x1 = gx1[i], ga = gv[i];
                z = __builtin_amdgcn_fdot2_f32_bf16(__builtin_bit_cast(bf16x2, x0), __builtin_bit_cast(bf16x2, ga), z, false);
                z1 = __builtin_amdgcn_fdot2_f32_bf16(__builtin_bit_cast(bf16x2, x1), __builtin_bit_cast(bf16x2, ga), z1, false);
            }
            { auto t = __builtin_amdgcn_permlane32_swap(FU(z), FU(z1), false, false); z = UF(t[0]) + UF(t[1]); }
            { auto t = __builtin_amdgcn_permlane16_swap(FU(z), FU(z), false, false); z = UF(t[0]) + UF(t[1]); }
#define DPPADD(v, ctl) v += UF(__builtin_amdgcn_mov_dpp(FU(v), ctl, 0xf, 0xf, true))
            DPPADD(z, 0x140); DPPADD(z, 0x141); DPPADD(z, 0x4e); DPPADD(z, 0xb1);
            float g = bf16r(__builtin_amdgcn_rcpf(1.f + __expf(-bf16r(z))));
            PIN(g);
            const lds_cptr wt = (lds_cptr)lds + L_WO + ((fg >> 1) * 8 + (fr >> 2)) * 256 + wid * 64 + (fg & 1) * 32 + (fr & 3) * 8;
            const u32x2 wlo = __builtin_bit_cast(u32x2, lds_tr(wt)), whi = __builtin_bit_cast(u32x2, lds_tr(wt + 1024));
            st16(ws + WS_W + ((size_t)n32 * 64 + ks) * 1024 + l16, u32x4{wlo[0], wlo[1], whi[0], whi[1]});
            if ((lane & 31) == 0) __hip_atomic_store((gu32*)(ws + WS_G + (rt * 64 + hg * 8 + wid * 2 + (lane >> 5)) * 4), FU(g), RLX_AGENT);
            asm volatile("s_waitcnt vmcnt(2)" ::: "memory");
            lds_inc(lds, L_CTL + 24);
            __builtin_amdgcn_s_setprio(0);
        } else {
            f32x4 mc, mp;
#pragma unroll
            for (int j = 0; j < 4; ++j) {
                const int d = fr - 4 * fg - j;
                mc[j] = (d >= 0 && !(d & 1)) ? 0.f : -INFINITY;
                mp[j] = (d <= -2 && !(d & 1)) ? 0.f : -INFINITY;
            }
            PIN(mc); PIN(mp);
            asm volatile("s_waitcnt vmcnt(4)\n\ts_barrier\n\ts_waitcnt vmcnt(0)\n\ts_barrier" ::: "memory");
            __builtin_amdgcn_s_setprio(2);
            proj_pass<true, false>((lds_cptr)lds, xo, wo, 2 * WSLAB, aq, ak, av);
            u32x2 qp[4], kp[5], vp[5];
#pragma unroll
            for (int m = 0; m < 4; ++m) qp[m] = pack4(aq[m] * QSCALE);
#pragma unroll
            for (int m = 0; m < 5; ++m) { kp[m] = pack4(ak[m]); vp[m] = pack4(av[m]); }
            const bf16x8 ones = __builtin_bit_cast(bf16x8, u32x4{0x3f803f80u, 0x3f803f80u, 0x3f803f80u, 0x3f803f80u});
            const f32x4 zero = {};
            f32x4 sp[4], sc[4]; float mx[4]; u32x2 op[4];
#pragma unroll
            for (int m = 3; m >= 0; --m) {
                f32x4 mpm = mp;
                if (m == 0 && rt == 0) mpm = f32x4{-INFINITY, -INFINITY, -INFINITY, -INFINITY};
                sc[m] = MF16K16(kp[m + 1], qp[m], mc); sp[m] = MF16K16(kp[m], qp[m], mpm);
            }
#pragma unroll
            for (int m = 3; m >= 0; --m)
                mx[m] = MX(MX(MX(MX(MX(MX(MX(sc[m][0], sc[m][1]), sc[m][2]), sc[m][3]), sp[m][0]), sp[m][1]), sp[m][2]), sp[m][3]);
            xmax4x4(mx);
            f32x4 o[4], l[4];
#pragma unroll
            for (int m = 0; m < 4; ++m) {
#pragma unroll
                for (int j = 0; j < 4; ++j) { sp[m][j] = __builtin_amdgcn_exp2f(sp[m][j] - mx[m]); sc[m][j] = __builtin_amdgcn_exp2f(sc[m][j] - mx[m]); }
                const u32x2 pp = pack4(sp[m]), pc2 = pack4(sc[m]);
                const bf16x8 pb = __builtin_bit_cast(bf16x8, u32x4{pp[0], pp[1], pc2[0], pc2[1]});
                const bf16x8 va = __builtin_bit_cast(bf16x8, u32x4{vp[m][0], vp[m][1], vp[m + 1][0], vp[m + 1][1]});
                o[m] = MF16(va, pb, zero); l[m] = MF16(ones, pb, zero);
            }
#pragma unroll
            for (int m = 0; m < 4; ++m) op[m] = pack4(o[m] * __builtin_amdgcn_rcpf(l[m][0]));
#pragma unroll
            for (int i = 0; i < 2; ++i) st16(ws + WS_O + (size_t)(rt * 2 + i) * 65536 + h1 * 1024 + l16, join2(op[2 * i], op[2 * i + 1]));
            __builtin_amdgcn_s_setprio(1);
        }
        lds_wait(lds, L_CTL + 24, 4);
    }

    {
        const int r32 = lane & 31, hh = lane >> 5;
        const int qt = qb * 8 + wid;
        const int vl = hh * 512 + (r32 & 15) * 16;
        f32x16 o; bf16x8 qf;
        {
            float o0, o1, o2, o3, o4, o5, o6, o7, o8;
            unsigned kpa = lds0 + L_KV + l16, vpa = lds0 + L_KV + 8192 + vl, cnt = 8;
            const unsigned qa = lds0 + L_Q + wid * 1024 + l16, cv = (lane & 27) == 16 ? 0x3f803f80u : 0u;
#define VM_ON "s_mov_b64 exec, %[em]\n\t"
#define VM_OFF "s_mov_b64 exec, -1\n\t"
#define XE(b, i) "v_exp_f32 v[" #b "+" #i "], v[" #b "+" #i "]\n\t"
#define ASTEP(cur, nxt, uv, uk, c4, n4) \
            XE(cur, 0) XE(cur, 1) \
            "v_smfmac_f32_32x32x32_bf16 v[64:79], v[112+" #n4 ":115+" #n4 "], v[" #nxt "+8:" #nxt "+15], v120\n\t" \
            VM_ON \
            "ds_read_b128 v[104+" #n4 ":107+" #n4 "], %[vp] offset:" #uv "*1024\n\t" \
            "ds_read_b128 v[112+" #n4 ":115+" #n4 "], %[vp] offset:" #uv "*1024+256\n\t" \
            VM_OFF \
            XE(cur, 2) XE(cur, 3) XE(cur, 4) XE(cur, 5) \
            "s_waitcnt lgkmcnt(2)\n\t" \
            "v_mfma_f32_32x32x16_bf16 v[" #nxt ":" #nxt "+15], v[96+" #n4 ":99+" #n4 "], %[q], v[80:95]\n\t" \
            "ds_read_b128 v[96+" #c4 ":99+" #c4 "], %[kp] offset:" #uk "*1024\n\t" \
            XE(cur, 6) XE(cur, 7) XE(cur, 8) XE(cur, 9) XE(cur, 10) XE(cur, 11) \
            "v_smfmac_f32_32x32x32_bf16 v[64:79], v[104+" #c4 ":107+" #c4 "], v[" #cur ":" #cur "+7], v120\n\t" \
            XE(cur, 12) XE(cur, 13) XE(cur, 14) XE(cur, 15)
#define XM(r, v) "v_mov_b32 v" #r ", " v "\n\t"
#define XM4(a, b, c, d, v) XM(a, v) XM(b, v) XM(c, v) XM(d, v)
#define XX3(a, b) "v_maximum3_f32 v121, v121, v" #a ", v" #b "\n\t"
            asm volatile(
                "s_waitcnt lgkmcnt(0)\n\t"
                "ds_read_b128 %[q], %[qa]\n\t"
                "ds_read_b128 v[96:99], %[kp]\n\t"
                XM4(104, 105, 106, 107, "%[cv]") XM4(112, 113, 114, 115, "%[cv]")
                VM_ON
                "ds_read_b128 v[104:107], %[vp]\n\t"
                "ds_read_b128 v[112:115], %[vp] offset:256\n\t"
                VM_OFF
                "ds_read_b128 v[100:103], %[kp] offset:1024\n\t"
                XM4(108, 109, 110, 111, "%[cv]") XM4(116, 117, 118, 119, "%[cv]") XM4(56, 57, 58, 59, "0") XM4(60, 61, 62, 63, "0")
                "s_waitcnt lgkmcnt(3)\n\t"
                "v_mfma_f32_32x32x16_bf16 v[32:47], v[96:99], %[q], 0\n\t"
                XM4(64, 65, 66, 67, "0") XM4(68, 69, 70, 71, "0") XM4(72, 73, 74, 75, "0") XM4(76, 77, 78, 79, "0") XM(120, "0xdddd")
                "v_maximum3_f32 v121, v32, v33, v34\n\t"
                XX3(35, 36) XX3(37, 38) XX3(39, 40) XX3(41, 42) XX3(43, 44) XX3(45, 46) XX3(47, 47)
                "v_mov_b32 v122, v121\n\t"
                "s_nop 1\n\t"
                "v_permlane32_swap_b32 v121, v122\n\t"
                "v_maximum3_f32 v121, v121, v122, v122\n\t"
                "v_sub_f32 v80, 0xc2700000, v121\n\t"
                XM(81, "v80") XM(82, "v80") XM(83, "v80") XM4(84, 85, 86, 87, "v80") XM4(88, 89, 90, 91, "v80") XM4(92, 93, 94, 95, "v80")
                "s_nop 1\n\t"
                "v_mfma_f32_32x32x16_bf16 v[32:47], v[96:99], %[q], v[80:95]\n\t"
                "s_nop 11\n\t"
                "1:\n\t"
                ASTEP(32, 48, 1, 2, 0, 4) ASTEP(48, 32, 2, 3, 4, 0) ASTEP(32, 48, 3, 4, 0, 4) ASTEP(48, 32, 4, 5, 4, 0) ASTEP(32, 48, 5, 6, 0, 4) ASTEP(48, 32, 6, 7, 4, 0)
                "s_waitcnt vmcnt(0)\n\t"
                "s_barrier\n\t"
                "v_add_u32 %[kp], 0x4000, %[kp]\n\t"
                ASTEP(32, 48, 7, 0, 0, 4)
                "v_add_u32 %[vp], 0x4000, %[vp]\n\t"
                ASTEP(48, 32, 0, 1, 4, 0)
                "s_sub_u32 %[cnt], %[cnt], 1\n\t"
                "s_cmp_lg_u32 %[cnt], 0\n\t"
                "s_cbranch_scc1 1b\n\t"
                "v_smfmac_f32_32x32x32_bf16 v[64:79], v[116:119], v[56:63], v120\n\t"
                "s_waitcnt lgkmcnt(0)\n\t"
                "s_nop 10\n\t"
                "v_mov_b32 %[o0], v64\n\tv_mov_b32 %[o1], v65\n\tv_mov_b32 %[o2], v66\n\tv_mov_b32 %[o3], v67\n\t"
                "v_mov_b32 %[o4], v68\n\tv_mov_b32 %[o5], v69\n\tv_mov_b32 %[o6], v70\n\tv_mov_b32 %[o7], v71\n\tv_mov_b32 %[o8], v72\n\t"
                : [o0] "=&v"(o0), [o1] "=&v"(o1), [o2] "=&v"(o2), [o3] "=&v"(o3), [o4] "=&v"(o4), [o5] "=&v"(o5), [o6] "=&v"(o6), [o7] "=&v"(o7), [o8] "=&v"(o8),
                  [q] "=&v"(qf), [kp] "+v"(kpa), [vp] "+v"(vpa), [cnt] "+s"(cnt)
                : [qa] "v"(qa), [cv] "v"(cv), [em] "s"(0x0000ffff0000ffffull)
                : "memory", "scc",
                  "v32", "v33", "v34", "v35", "v36", "v37", "v38", "v39", "v40", "v41", "v42", "v43", "v44", "v45", "v46", "v47",
                  "v48", "v49", "v50", "v51", "v52", "v53", "v54", "v55", "v56", "v57", "v58", "v59", "v60", "v61", "v62", "v63",
                  "v64", "v65", "v66", "v67", "v68", "v69", "v70", "v71", "v72", "v73", "v74", "v75", "v76", "v77", "v78", "v79",
                  "v80", "v81", "v82", "v83", "v84", "v85", "v86", "v87", "v88", "v89", "v90", "v91", "v92", "v93", "v94", "v95",
                  "v96", "v97", "v98", "v99", "v100", "v101", "v102", "v103", "v104", "v105", "v106", "v107", "v108", "v109", "v110", "v111",
                  "v112", "v113", "v114", "v115", "v116", "v117", "v118", "v119", "v120", "v121", "v122");
            o[0] = o0; o[1] = o1; o[2] = o2; o[3] = o3; o[4] = o4; o[5] = o5; o[6] = o6; o[7] = o7; o[8] = o8;
        }
        {
            float chk = MX(fabsf(o[0]), fabsf(o[1]));
#pragma unroll
            for (int r = 2; r < 8; r += 2) chk = MX(MX(chk, fabsf(o[r])), fabsf(o[r + 1]));
            if (__any(!(MX(chk, o[8]) < 1e36f) || chk < 1e-30f)) {
                const __amdgpu_buffer_rsrc_t rs = __builtin_amdgcn_make_buffer_rsrc(ws + WS_K + hB, 0, (2u << 20) + 65536, 0x00020000);
                const u32x4 ones = {0x3f803f80u, 0x3f803f80u, 0x3f803f80u, 0x3f803f80u};
                float m = -INFINITY; o = f32x16{};
#pragma unroll 1
                for (int t = 0; t < 64; ++t) {
                    const u32x4 kf = __builtin_amdgcn_raw_buffer_load_b128(rs, l16, t * 1024, 16);
                    u32x4 v0 = __builtin_amdgcn_raw_buffer_load_b128(rs, vl, (2u << 20) + t * 1024, 16);
                    u32x4 v1 = __builtin_amdgcn_raw_buffer_load_b128(rs, vl + 256, (2u << 20) + t * 1024, 16);
                    if (r32 >= 16) { v0 = ones; v1 = ones; }
                    f32x16 z = {}; f32x16 s = MF32(BF8(kf), qf, z);
                    float tm = s[0];
#pragma unroll
                    for (int r = 1; r < 16; ++r) tm = MX(tm, s[r]);
                    const float mn = MX(m, halfmax(tm));
                    const float al = __builtin_amdgcn_exp2f(m - mn); m = mn;
#pragma unroll
                    for (int r = 0; r < 16; ++r) { s[r] = __builtin_amdgcn_exp2f(s[r] - mn); o[r] *= al; }
                    o = SMF(BF8(v0), LO8(s), o); o = SMF(BF8(v1), HI8(s), o);
                }
            }
        }
        {
            const float sc = __builtin_amdgcn_rcpf(o[8]);
            const unsigned a0 = cvtpk(o[0] * sc, o[1] * sc), a1 = cvtpk(o[2] * sc, o[3] * sc);
            const unsigned b0 = cvtpk(o[4] * sc, o[5] * sc), b1 = cvtpk(o[6] * sc, o[7] * sc);
            auto r0 = __builtin_amdgcn_permlane32_swap(a0, b0, false, false);
            auto r1 = __builtin_amdgcn_permlane32_swap(a1, b1, false, false);
            st16(ws + WS_O + (size_t)qt * 65536 + (NH + head) * 1024 + l16, u32x4{r0[0], r1[0], r0[1], r1[1]});
        }
    }

    {
        const int mt = wid >> 2, nt = wid & 3, rl = mt * 32 + fr;
        const lds_cptr wp = (lds_cptr)lds + L_KV + (nt >> 1) * 32768 + fg * 512 + ((nt & 1) * 16 + fr) * 16;
        unsigned long long tag2 = *(volatile LAS unsigned long long*)((lds_ptr)lds + L_CTL);
        char* fdst = ws + WS_F2 + ((qb * 4 + (wid >> 1)) * 8 + (lane & 7)) * F2S + (head * 2 + (wid & 1)) * 8;
        PIN(tag2); PIN(fdst);
        u32x4 wf[16];
#pragma unroll
        for (int k = 0; k < 16; ++k) wf[k] = *(const LAS u32x4*)(wp + k * 2048);
        lds_inc(lds, L_CTL + 28);
        asm volatile("s_waitcnt vmcnt(0)" ::: "memory");
        if (lane < 8) __hip_atomic_store((gu64*)fdst, tag2, RLX_AGENT);
        char* dst = (char*)out + (size_t)(r64 * 64 + rl + (fg & 1) * 16) * 1024 + (nb * 64 + nt * 16 + (fg >> 1) * 8) * 2;
        PIN(dst);
        f32x4 e0[4] = {}, e1[4] = {};
        const unsigned oa = lds0 + L_KV + mt * 32768 + fg * 512 + fr * 16;
#define P3R(r, g, o) "ds_read_b128 %" #r ", %[o] offset:" #g "*8192+" #o "\n\t"
#define P3M(n, w, a, b, c0, c1) "s_waitcnt lgkmcnt(" #n ")\n\tv_mfma_f32_16x16x32_bf16 %0, %[" #w "], %" #a ", " c0 "\n\tv_mfma_f32_16x16x32_bf16 %1, %[" #w "], %" #b ", " c1 "\n\t"
#define P3G(g) do { u32x4 t0, t1, t2, t3, t4, t5, t6, t7; asm volatile( \
            "s_bitcmp0_b32 %[m], " #g "\n\ts_cbranch_scc1 1f\n\t" \
            P3R(2, g, 0) P3R(3, g, 256) P3R(4, g, 2048) P3R(5, g, 2304) P3R(6, g, 4096) P3R(7, g, 4352) P3R(8, g, 6144) P3R(9, g, 6400) \
            P3M(6, w0, 2, 3, "0", "0") P3M(4, w1, 4, 5, "%0", "%1") P3M(2, w2, 6, 7, "%0", "%1") P3M(0, w3, 8, 9, "%0", "%1") "1:" \
            : "+v"(e0[g]), "+v"(e1[g]), "=&v"(t0), "=&v"(t1), "=&v"(t2), "=&v"(t3), "=&v"(t4), "=&v"(t5), "=&v"(t6), "=&v"(t7) \
            : [m] "s"(m), [o] "v"(oa), [w0] "v"(wf[4 * g]), [w1] "v"(wf[4 * g + 1]), [w2] "v"(wf[4 * g + 2]), [w3] "v"(wf[4 * g + 3]) : "memory", "scc"); } while (0)
        volatile LAS unsigned* arrived = (volatile LAS unsigned*)((lds_ptr)lds + L_CTL + 12);
        lds_wait(lds, L_CTL + 32, 4);
        const u32x2 pa0 = *(const LAS u32x2*)((lds_cptr)lds + L_PA + rl * PA_LD + nt * 32 + fg * 8);
        const u32x2 pa1 = *(const LAS u32x2*)((lds_cptr)lds + L_PA + (rl + 16) * PA_LD + nt * 32 + fg * 8);
        float g0 = *(const LAS float*)((lds_cptr)lds + L_GT + rl * 4), g1 = *(const LAS float*)((lds_cptr)lds + L_GT + rl * 4 + 64);
        f32x4 p0 = g0 * f32x4{bflo(pa0[0]), bfhi(pa0[0]), bflo(pa0[1]), bfhi(pa0[1])}, p1 = g1 * f32x4{bflo(pa1[0]), bfhi(pa1[0]), bflo(pa1[1]), bfhi(pa1[1])};
        g0 = bf16r(1.f - g0); g1 = bf16r(1.f - g1);
        PIN(g0); PIN(g1); PIN(p0); PIN(p1);
        unsigned done = 0;
#pragma unroll 1
        do {
            unsigned m;
            while (!(m = __builtin_amdgcn_readfirstlane(*arrived) & ~done)) __builtin_amdgcn_s_sleep(1);
            asm volatile("s_waitcnt lgkmcnt(0)" ::: "memory");
            P3G(0); P3G(1); P3G(2); P3G(3);
            done |= m;
        } while (done != 15u);
        asm volatile("s_nop 7" : "+v"(e0[3]), "+v"(e1[3]), "+v"(e0[2]), "+v"(e1[2]), "+v"(e0[1]), "+v"(e1[1]), "+v"(e0[0]), "+v"(e1[0]));
        const f32x4 d0 = (e0[0] + e0[1]) + (e0[2] + e0[3]), d1 = (e1[0] + e1[1]) + (e1[2] + e1[3]);
        *(__attribute__((address_space(1))) u32x4*)dst = join2(pack4(p0 + g0 * d0), pack4(p1 + g1 * d1));
    }
}

extern "C" void kernel_launch(void* const* d_in, const int* in_sizes, int n_in, void* d_out, int out_size, void* d_ws,
                              size_t ws_size, hipStream_t stream) {
    (void)hipFuncSetAttribute((const void*)fused, hipFuncAttributeMaxDynamicSharedMemorySize, LDS_BYTES);
    fused<<<256, 768, LDS_BYTES, stream>>>((const __hip_bfloat16*)d_in[0], (const __hip_bfloat16*)d_in[1], (const __hip_bfloat16*)d_in[4],
                                           (const __hip_bfloat16*)d_in[2], (const __hip_bfloat16*)d_in[5], (const __hip_bfloat16*)d_in[6],
                                           (char*)d_ws, (__hip_bfloat16*)d_out);
}
```
